```python
import torch

def _softmax(x, axis=-1):
    return torch.softmax(x, dim=axis)
def _gelu(x):
    return torch.nn.functional.gelu(x, approximate="tanh")
def _silu(x):
    return torch.nn.functional.silu(x)
def _sigmoid(x):
    return torch.sigmoid(x)
def _layernorm(x, axis=-1, eps=1e-5):
    m = x.mean(dim=axis, keepdim=True)
    v = x.var(dim=axis, keepdim=True, unbiased=False)
    return (x - m) / torch.sqrt(v + eps)
def _scatter_add(tgt, idx, src, axis=0):
    return tgt.clone().index_add_(axis, idx, src)
def _segment_sum(src, seg, n):
    out = torch.zeros((n,) + src.shape[1:], dtype=src.dtype)
    return out.index_add_(0, seg, src)
def _pad(x, pw):
    flat = tuple(v for pair in reversed(pw) for v in pair)
    return torch.nn.functional.pad(x, flat)

kernel_name = 'parallel_sum__grouped_kv_mqa__dense_activation_relu__prompt'

def setup_inputs():

    _w = (lambda a: (lambda t: t.to(torch.bfloat16) if t.is_floating_point() else t)(torch.as_tensor(a)))
    def _A_setup():
        B, S, D, H = 1, 2048, 512, 32
        Dh = D // H
        return {
            "x": _w((lambda *s: torch.randn(*s, dtype=torch.bfloat16) * 0.05)(B, S, D)),
            "wq": _w((lambda *s: torch.randn(*s, dtype=torch.bfloat16) * 0.2)(D, H * Dh)),
            "wk": _w((lambda *s: torch.randn(*s, dtype=torch.bfloat16) * 0.2)(D, Dh)),
            "wv": _w((lambda *s: torch.randn(*s, dtype=torch.bfloat16) * 0.1)(D, Dh)),
            "wo": _w((lambda *s: torch.randn(*s, dtype=torch.bfloat16) * 0.1)(H * Dh, D)),
        }
    _A_in = _A_setup()

    def _B_setup():
        B, S, D, Dff = 1, 2048, 512, 1376
        return {
            "x": _w((lambda *s: torch.randn(*s, dtype=torch.bfloat16) * 0.05)(B, S, D)),
            "w1": _w((lambda *s: torch.randn(*s, dtype=torch.bfloat16) * 0.1)(D, Dff)),
            "w2": _w((lambda *s: torch.randn(*s, dtype=torch.bfloat16) * 0.1)(Dff, D)),
        }
    _B_in = _B_setup()

    return {
        "A_x": _A_in["x"],
        "A_wq": _A_in["wq"],
        "A_wk": _A_in["wk"],
        "A_wv": _A_in["wv"],
        "A_wo": _A_in["wo"],
        "B_x": _B_in["x"],
        "B_w1": _B_in["w1"],
        "B_w2": _B_in["w2"],
    }

def reference(A_x, A_wq, A_wk, A_wv, A_wo, B_x, B_w1, B_w2):
    def _A_ref(x, wq, wk, wv, wo):
        B, S, D = x.shape
        H = 32
        Dh = D // H
        q = (lambda x, *d: x.permute(*d))((x @ wq).reshape(B, S, H, Dh), 0, 2, 1, 3)
        k = x @ wk
        v = x @ wv
        scores = torch.einsum("bhqd,bkd->bhqk", q, k) * (Dh ** -0.5)
        p = _softmax(scores, axis=-1)
        o = torch.einsum("bhqk,bkd->bhqd", p, v)
        return (lambda x, *d: x.permute(*d))(o, 0, 2, 1, 3).reshape(B, S, H * Dh) @ wo

    def _B_ref(x, w1, w2):
        h = torch.einsum("bsd,df->bsf", x, w1)
        zero = torch.zeros_like(h)
        h = torch.maximum(h, zero)
        return torch.einsum("bsf,fd->bsd", h, w2)

    x = A_x
    xn = _layernorm(x, axis=-1)
    return x + _A_ref(xn, A_wq, A_wk, A_wv, A_wo) + _B_ref(xn, B_w1, B_w2)
```

```cpp
#include <hip/hip_runtime.h>
#include <hip/hip_bf16.h>
#include <stdint.h>

typedef __attribute__((ext_vector_type(8))) __bf16 bf16x8;
typedef __attribute__((ext_vector_type(2))) __bf16 bf16x2;
typedef __attribute__((ext_vector_type(16))) __bf16 bf16x16;
typedef __attribute__((ext_vector_type(16))) float f32x16;
typedef __attribute__((ext_vector_type(8))) float f32x8;
typedef __attribute__((ext_vector_type(2))) float f32x2;
typedef __attribute__((ext_vector_type(4))) unsigned u32x4;
typedef __attribute__((ext_vector_type(2))) unsigned u32x2;
typedef __attribute__((ext_vector_type(4))) short s16x4;
typedef __attribute__((ext_vector_type(8))) short s16x8;
#define LAS __attribute__((address_space(3)))
#define DEV __device__ __forceinline__

constexpr int SEQ = 2048, DM = 512, NH = 32, DH = 16, FF = 1376;
constexpr int KIMG = 65536, VSTEP = 528, VIMG = 128 * VSTEP, KVIMG = KIMG + VIMG;
constexpr size_t WS_Q = 0, WS_KV = 2u << 20, WS_O = WS_KV + (256u << 10), WS_H = WS_O + (2u << 20);
constexpr int HSTEPS = FF / 16;

DEV unsigned pk2(float lo, float hi) { f32x2 v = {lo, hi}; return __builtin_bit_cast(unsigned, __builtin_convertvector(v, bf16x2)); }
DEV float bflo(unsigned u) { return __uint_as_float(u << 16); }
DEV float bfhi(unsigned u) { return __uint_as_float(u & 0xffff0000u); }
DEV f32x16 mfma(bf16x8 a, bf16x8 b, f32x16 c) { return __builtin_amdgcn_mfma_f32_32x32x16_bf16(a, b, c, 0, 0, 0); }
DEV void glds16(const void* g, unsigned lds_addr) {
    unsigned keep;
    asm volatile("s_mov_b32 %0, m0\n\ts_mov_b32 m0, %2\n\ts_nop 2\n\tglobal_load_lds_dwordx4 %1, off\n\ts_mov_b32 m0, %0"
                 : "=&s"(keep) : "v"(g), "s"(lds_addr) : "memory");
}
DEV s16x4 tr_read(LAS const char* p) { return __builtin_amdgcn_ds_read_tr16_b64_v4i16((LAS s16x4*)p); }
DEV bf16x8 wfrag(LAS const char* base_lane  , int kstep) {
    s16x4 a = tr_read(base_lane + kstep * 1024), b = tr_read(base_lane + kstep * 1024 + 256);
    s16x8 r = {a[0], a[1], a[2], a[3], b[0], b[1], b[2], b[3]};
    return __builtin_bit_cast(bf16x8, r);
}
DEV unsigned wfrag_lane_off(int l) {
    int h = l >> 5, blk = (l >> 4) & 1, q = (l & 15) >> 2, p = l & 3;
    return (8 * h + q) * 64 + blk * 32 + p * 8;
}
DEV u32x4 widen(u32x2 a, u32x2 b) {
    auto r0 = __builtin_amdgcn_permlane32_swap(a[0], b[0], false, false);
    auto r1 = __builtin_amdgcn_permlane32_swap(a[1], b[1], false, false);
    u32x4 o = {r0[0], r1[0], r0[1], r1[1]};
    return o;
}

__global__ __launch_bounds__(512, 2) void kA(const uint16_t* __restrict__ x, const uint16_t* __restrict__ wq, const uint16_t* __restrict__ wk,
                                             const uint16_t* __restrict__ wv, const uint16_t* __restrict__ w1, char* __restrict__ ws) {
    __shared__ __attribute__((aligned(16))) char smem[65536 + 8 * 2 * 4096];
    const int tid = threadIdx.x, l = tid & 63, w = __builtin_amdgcn_readfirstlane(tid >> 6);
    const int b = blockIdx.x, xcd = b & 7, idx = b >> 3, rb = xcd * 4 + (idx >> 3), j = idx & 7;
    const int row0 = rb * 64;
    u32x4 xv[8];
    {
        const u32x4* xr = (const u32x4*)(x + (size_t)(row0 + w * 8 + (l >> 3)) * DM);
#pragma unroll
        for (int i = 0; i < 8; ++i) xv[i] = xr[(l & 7) + 8 * i];
    }
    const int hcount = j < 3 ? 6 : 5, hstart = j < 3 ? 6 * j : 18 + 5 * (j - 3);
    int type;
    const uint16_t* wp; int ldw;
    if (w < 2) { type = 0; wp = wq + 64 * j + 32 * w + (l & 3) * 8; ldw = DM; }
    else if (w - 2 < hcount) { type = 1; wp = w1 + (hstart + w - 2) * 32 + (l & 3) * 8; ldw = FF; }
    else if (j == 3 && w == 7) { type = 2; wp = ((l & 3) < 2 ? wk + (l & 3) * 8 : wv + ((l & 3) - 2) * 8); ldw = DH; }
    else { type = 3; wp = wq; ldw = 0; }
    wp += (size_t)(l >> 2) * ldw;
    const unsigned wst = (unsigned)(uintptr_t)(smem + 65536) + w * 8192;
    auto stage = [&](int c) {
#pragma unroll
        for (int i = 0; i < 4; ++i) glds16(wp + (size_t)(c * 64 + i * 16) * ldw, wst + (c & 1) * 4096 + i * 1024);
    };
    if (type != 3) { stage(0); stage(1); }
    {
        float f[64];
#pragma unroll
        for (int i = 0; i < 8; ++i)
#pragma unroll
            for (int e = 0; e < 4; ++e) { f[i * 8 + 2 * e] = bflo(xv[i][e]); f[i * 8 + 2 * e + 1] = bfhi(xv[i][e]); }
        float s = 0;
#pragma unroll
        for (int i = 0; i < 64; ++i) s += f[i];
        s += __shfl_xor(s, 1); s += __shfl_xor(s, 2); s += __shfl_xor(s, 4);
        const float mean = s * (1.f / DM);
        float v = 0;
#pragma unroll
        for (int i = 0; i < 64; ++i) { f[i] -= mean; v += f[i] * f[i]; }
        v += __shfl_xor(v, 1); v += __shfl_xor(v, 2); v += __shfl_xor(v, 4);
        const float rs = 1.0f / sqrtf(v * (1.f / DM) + 1e-5f);
        const int row = w * 8 + (l >> 3);
#pragma unroll
        for (int i = 0; i < 8; ++i) {
            u32x4 o;
#pragma unroll
            for (int e = 0; e < 4; ++e) o[e] = pk2(f[i * 8 + 2 * e] * rs, f[i * 8 + 2 * e + 1] * rs);
            const int ch = (l & 7) + 8 * i;
            *(LAS u32x4*)(LAS char*)(smem + row * 1024 + ((ch ^ (row & 15)) << 4)) = o;
        }
    }
    __syncthreads();
    if (type == 3) return;
    f32x16 acc0 = {}, acc1 = {}, acc2 = {}, acc3 = {};
    const int r32 = l & 31, h = l >> 5;
    LAS const char* xb = (LAS const char*)smem + r32 * 1024;
    const unsigned xo = (unsigned)((h ^ (r32 & 15)) << 4);
    LAS const char* wb = (LAS const char*)smem + 65536 + w * 8192 + wfrag_lane_off(l);
    for (int c = 0; c < 8; ++c) {
        if (c < 7) asm volatile("s_waitcnt vmcnt(4)" ::: "memory"); else asm volatile("s_waitcnt vmcnt(0)" ::: "memory");
#pragma unroll
        for (int ks = 0; ks < 4; ++ks) {
            const int kk = c * 4 + ks;
            const unsigned off = (xo ^ ((kk & 7) << 5)) + (kk >> 3) * 256;
            bf16x8 x0 = *(LAS const bf16x8*)(xb + off), x1 = *(LAS const bf16x8*)(xb + 32768 + off);
            bf16x8 wf = wfrag(wb + (c & 1) * 4096, ks);
            acc0 = mfma(wf, x0, acc0); acc1 = mfma(wf, x1, acc1);
            if (type == 2) { acc2 = mfma(x0, wf, acc2); acc3 = mfma(x1, wf, acc3); }
        }
        if (c + 2 < 8) { asm volatile("s_waitcnt lgkmcnt(0)" ::: "memory"); stage(c + 2); }
    }
    if (type == 0) {
        const float sc = 0.25f * 1.4426950408889634f;
        uint16_t* q = (uint16_t*)(ws + WS_Q);
#pragma unroll
        for (int rt = 0; rt < 2; ++rt) {
            const f32x16& a = rt ? acc1 : acc0;
#pragma unroll
            for (int p = 0; p < 2; ++p) {
                u32x2 g0 = {pk2(a[8 * p] * sc, a[8 * p + 1] * sc), pk2(a[8 * p + 2] * sc, a[8 * p + 3] * sc)};
                u32x2 g1 = {pk2(a[8 * p + 4] * sc, a[8 * p + 5] * sc), pk2(a[8 * p + 6] * sc, a[8 * p + 7] * sc)};
                u32x4 o = widen(g0, g1);
                *(u32x4*)(q + (size_t)(row0 + rt * 32 + r32) * DM + 64 * j + 32 * w + 16 * p + 8 * h) = o;
            }
        }
    } else if (type == 1) {
        char* hb = ws + WS_H;
        const int t = hstart + w - 2;
#pragma unroll
        for (int rt = 0; rt < 2; ++rt) {
            const f32x16& a = rt ? acc1 : acc0;
#pragma unroll
            for (int p = 0; p < 2; ++p) {
                u32x2 g0 = {pk2(fmaxf(a[8 * p], 0.f), fmaxf(a[8 * p + 1], 0.f)), pk2(fmaxf(a[8 * p + 2], 0.f), fmaxf(a[8 * p + 3], 0.f))};
                u32x2 g1 = {pk2(fmaxf(a[8 * p + 4], 0.f), fmaxf(a[8 * p + 5], 0.f)), pk2(fmaxf(a[8 * p + 6], 0.f), fmaxf(a[8 * p + 7], 0.f))};
                u32x4 o = widen(g0, g1);
                *(u32x4*)(hb + ((size_t)(rb * 2 + rt) * HSTEPS + 2 * t + p) * 1024 + l * 16) = o;
            }
        }
    } else {
        char* kv = ws + WS_KV;
#pragma unroll
        for (int rt = 0; rt < 2; ++rt) {
            const int tile = rb * 2 + rt;
            const f32x16& a = rt ? acc1 : acc0;
#pragma unroll
            for (int i = 0; i < 2; ++i) {
                u32x2 g = {pk2(a[4 * i], a[4 * i + 1]), pk2(a[4 * i + 2], a[4 * i + 3])};
                *(u32x2*)(kv + tile * 1024 + i * 512 + r32 * 16 + 8 * h) = g;
            }
            const f32x16& c2 = rt ? acc3 : acc2;
            if (r32 >= 16) {
#pragma unroll
                for (int i = 0; i < 4; ++i) {
                    u32x2 g = {pk2(c2[4 * i], c2[4 * i + 1]), pk2(c2[4 * i + 2], c2[4 * i + 3])};
                    *(u32x2*)(kv + KIMG + (2 * tile + (i >> 1)) * VSTEP + (i & 1) * 256 + (r32 - 16) * 16 + 8 * h) = g;
                }
            } else if (r32 < 2 && h == 0) {
                u32x4 ones = {0x3F803F80u, 0x3F803F80u, 0x3F803F80u, 0x3F803F80u};
                *(u32x4*)(kv + KIMG + (2 * tile + r32) * VSTEP + 512) = ones;
            }
        }
    }
}

constexpr int SCR = KVIMG;
constexpr int SCR_BYTES = 8 * 9 * 256 + 16;
DEV bf16x16 lo8(const f32x16& s) { return __builtin_bit_cast(bf16x16, __builtin_shufflevector(s, s, 0, 1, 2, 3, 4, 5, 6, 7)); }
DEV bf16x16 hi8(const f32x16& s) { return __builtin_bit_cast(bf16x16, __builtin_shufflevector(s, s, 8, 9, 10, 11, 12, 13, 14, 15)); }
DEV float halfmax(float m) { auto r = __builtin_amdgcn_permlane32_swap(__float_as_uint(m), __float_as_uint(m), false, false); return fmaxf(__uint_as_float(r[0]), __uint_as_float(r[1])); }
DEV float max16(const f32x16& s, float m) {
#pragma unroll
    for (int i = 0; i < 16; i += 2) m = fmaxf(fmaxf(m, s[i]), s[i + 1]);
    return m;
}


#define EX(r) "v_exp_f32 v" #r ", v" #r "\n\t"
#define WL2 "s_waitcnt lgkmcnt(2)\n\t"
#define QK(d0, d1) "v_mfma_f32_32x32x16_bf16 v[" #d0 ":" #d1 "], v[84:87], v[80:83], v[32:47]\n\t"
#define PV(vf0, vf1, b0, b1) "v_smfmac_f32_32x32x32_bf16 v[16:31], v[" #vf0 ":" #vf1 "], v[" #b0 ":" #b1 "], v96\n\t"
#define RDK(off) "ds_read_b128 v[84:87], v97 offset:" #off "\n\t"
#define RDV0(off) "ds_read_b128 v[88:91], v98 offset:" #off "\n\t"
#define RDV1(off) "ds_read_b128 v[92:95], v98 offset:" #off "\n\t"
#define PHASE_A(ko, vo0, vo1)   \
    EX(50) EX(51) EX(52) EX(53) WL2 QK(64, 79) RDK(ko) EX(54) EX(55) EX(56) EX(57) WL2 PV(88, 91, 48, 55) RDV0(vo0) \
    EX(58) EX(59) EX(60) EX(61) EX(62) EX(63) EX(64) EX(65) WL2 PV(92, 95, 56, 63) RDV1(vo1)
#define PHASE_B(ko, vo0, vo1)   \
    EX(66) EX(67) EX(68) EX(69) WL2 QK(48, 63) RDK(ko) EX(70) EX(71) EX(72) EX(73) WL2 PV(88, 91, 64, 71) RDV0(vo0) \
    EX(74) EX(75) EX(76) EX(77) EX(78) EX(79) EX(48) EX(49) WL2 PV(92, 95, 72, 79) RDV1(vo1)
DEV void attn_loop(f32x16& o, const f32x16& cin, const bf16x8& q, int idx, unsigned kaddr, unsigned vaddr) {
    int cnt = 8;
    asm volatile(
        "s_nop 1\n\t"
        RDK(0)
        "s_waitcnt lgkmcnt(0)\n\t"
        QK(48, 63)
        RDK(1024) RDV0(0) RDV1(528)
        "s_nop 8\n\t"
        EX(48) EX(49)
        "1:\n\t"
        PHASE_A(2048, 1056, 1584)
        PHASE_B(3072, 2112, 2640)
        PHASE_A(4096, 3168, 3696)
        PHASE_B(5120, 4224, 4752)
        "v_add_u32 v97, 0x1000, v97\n\t"
        "v_add_u32 v98, 0x1080, v98\n\t"
        "s_sub_u32 %[cnt], %[cnt], 1\n\t"
        "s_cmp_lg_u32 %[cnt], 0\n\t"
        "s_cbranch_scc1 1b\n\t"
        "s_waitcnt lgkmcnt(0)\n\t"
        "s_nop 15\n\t"
        : "+{v[16:31]}"(o), "+{v97}"(kaddr), "+{v98}"(vaddr), [cnt] "+s"(cnt)
        : "{v[32:47]}"(cin), "{v[80:83]}"(q), "{v96}"(idx)
        : "v48", "v49", "v50", "v51", "v52", "v53", "v54", "v55", "v56", "v57", "v58", "v59", "v60", "v61", "v62", "v63",
          "v64", "v65", "v66", "v67", "v68", "v69", "v70", "v71", "v72", "v73", "v74", "v75", "v76", "v77", "v78", "v79",
          "v84", "v85", "v86", "v87", "v88", "v89", "v90", "v91", "v92", "v93", "v94", "v95", "scc", "memory");
}

__global__ __launch_bounds__(1024, 4) void kB(char* __restrict__ ws) {
    __shared__ __attribute__((aligned(16))) char smem[KVIMG + SCR_BYTES];
    const int tid = threadIdx.x, l = tid & 63, w = __builtin_amdgcn_readfirstlane(tid >> 6);
    const int b = blockIdx.x, xcd = b & 7, idx = b >> 3, rb = xcd * 4 + (idx >> 3), j = idx & 7;
    const int cb = w & 7, kh = w >> 3, head = 4 * j + (cb & 3), qh = cb >> 2;
    const int r32 = l & 31, h = l >> 5;
    const unsigned sm0 = (unsigned)(uintptr_t)smem;
    {
        const char* src = ws + WS_KV + l * 16;
        for (int i = w; i < KVIMG / 1024; i += 16) glds16(src + i * 1024, sm0 + i * 1024);
    }
    const bf16x8 q = *(const bf16x8*)((const uint16_t*)(ws + WS_Q) + (size_t)(rb * 64 + qh * 32 + r32) * DM + head * 16 + 8 * h);
    if (tid == 0) *(LAS unsigned*)(LAS char*)(smem + SCR + 8 * 9 * 256) = 0;
    asm volatile("s_waitcnt vmcnt(0)" ::: "memory");
    __syncthreads();
    LAS const char* kb = (LAS const char*)smem + l * 16;
    LAS const char* vb = (LAS const char*)smem + KIMG + (r32 < 16 ? h * 256 + r32 * 16 : 512);
    const int idxv = 0xDDDD;
    float mref;
    {
        float m = -3.0e38f; const f32x16 z = {};
#pragma unroll
        for (int t = 0; t < 4; ++t) { f32x16 s = mfma(*(LAS const bf16x8*)(kb + t * 1024), q, z); m = max16(s, m); }
        mref = halfmax(m) + 64.f;
    }
    for (int attempt = 0;; ++attempt) {
        f32x16 cin, o = {};
#pragma unroll
        for (int i = 0; i < 16; ++i) cin[i] = -mref;
        const int t0 = kh * 32;
        attn_loop(o, cin, q, idxv, (unsigned)(uintptr_t)(kb + t0 * 1024), (unsigned)(uintptr_t)(vb + t0 * 2 * VSTEP));
        LAS float* sc = (LAS float*)(LAS char*)(smem + SCR) + cb * 9 * 64 + l;
        if (kh == 1) {
#pragma unroll
            for (int i = 0; i < 9; ++i) sc[i * 64] = o[i];
        }
        __syncthreads();
        bool bad = false;
        if (kh == 0) {
#pragma unroll
            for (int i = 0; i < 9; ++i) o[i] += sc[i * 64];
            const float lsum = o[8];
            bad = !(lsum < 1.0e30f) || !(lsum > 0.f);
#pragma unroll
            for (int i = 0; i < 8; ++i) bad = bad || !(fabsf(o[i]) < 3.0e38f);
            if (attempt == 0 && __any(bad)) { if (l == 0) *(LAS unsigned*)(LAS char*)(smem + SCR + 8 * 9 * 256) = 1; }
            if (attempt > 0 || !__any(bad)) {
                const float r = 1.0f / lsum;
                u32x2 g0 = {pk2(o[0] * r, o[1] * r), pk2(o[2] * r, o[3] * r)}, g1 = {pk2(o[4] * r, o[5] * r), pk2(o[6] * r, o[7] * r)};
                *(u32x4*)(ws + WS_O + ((size_t)(rb * 2 + qh) * 32 + head) * 1024 + l * 16) = widen(g0, g1);
            }
        }
        if (attempt > 0) break;
        __syncthreads();
        if (*(LAS volatile unsigned*)(LAS char*)(smem + SCR + 8 * 9 * 256) == 0) break;
        float m = -3.0e38f; const f32x16 z = {};
        for (int t = 0; t < 64; ++t) { f32x16 s = mfma(*(LAS const bf16x8*)(kb + t * 1024), q, z); m = max16(s, m); }
        mref = halfmax(m);
    }
}

constexpr int CRING = 6, CSTG = 3072;
__global__ __launch_bounds__(512, 2) void kC(const uint16_t* __restrict__ x, const uint16_t* __restrict__ wo, const uint16_t* __restrict__ w2,
                                             const char* __restrict__ ws, uint16_t* __restrict__ out) {
    __shared__ __attribute__((aligned(16))) char smem[8 * CRING * CSTG];
    const int tid = threadIdx.x, l = tid & 63, w = __builtin_amdgcn_readfirstlane(tid >> 6);
    const int b = blockIdx.x, xcd = b & 7, idx = b >> 3, rb = xcd * 4 + (idx >> 3), j = idx & 7;
    const int nt = w & 1, kq = w >> 1, r32 = l & 31, h = l >> 5;
    const int col0 = 64 * j + 32 * nt;
    int nk; const char* ap; const uint16_t* wp; size_t astride;
    if (kq == 0) { nk = 32; ap = ws + WS_O + (size_t)(rb * 2) * 32 * 1024; astride = 32 * 1024; wp = wo + col0; }
    else { const int k0 = (kq - 1) * 29; nk = kq == 3 ? 28 : 29; ap = ws + WS_H + ((size_t)(rb * 2) * HSTEPS + k0) * 1024; astride = (size_t)HSTEPS * 1024; wp = w2 + (size_t)k0 * 16 * DM + col0; }
    ap += l * 16; wp += (size_t)(l >> 2) * DM + (l & 3) * 8;
    const unsigned st = (unsigned)(uintptr_t)smem + w * CRING * CSTG;
    auto stage = [&](int k, int slot) {
        glds16(ap + (size_t)k * 1024, st + slot * CSTG);
        glds16(ap + astride + (size_t)k * 1024, st + slot * CSTG + 1024);
        glds16(wp + (size_t)k * 16 * DM, st + slot * CSTG + 2048);
    };
#pragma unroll
    for (int k = 0; k < CRING - 1; ++k) stage(k, k);
    f32x16 acc0 = {}, acc1 = {};
    LAS const char* sb = (LAS const char*)smem + w * CRING * CSTG;
    const unsigned wl = wfrag_lane_off(l);
    int slot = 0, nslot = CRING - 1;
    for (int k = 0; k < nk; ++k) {
        asm volatile("s_waitcnt vmcnt(12)" ::: "memory");
        LAS const char* p = sb + slot * CSTG;
        bf16x8 a0 = *(LAS const bf16x8*)(p + l * 16), a1 = *(LAS const bf16x8*)(p + 1024 + l * 16);
        bf16x8 wf = wfrag(p + 2048 + wl, 0);
        acc0 = mfma(wf, a0, acc0); acc1 = mfma(wf, a1, acc1);
        asm volatile("s_waitcnt lgkmcnt(0)" ::: "memory");
        const int kn = k + CRING - 1;
        stage(kn < nk ? kn : nk - 1, nslot);
        slot = slot + 1 == CRING ? 0 : slot + 1; nslot = nslot + 1 == CRING ? 0 : nslot + 1;
    }
    asm volatile("s_waitcnt vmcnt(0)" ::: "memory");
    __syncthreads();
    LAS float* red = (LAS float*)(LAS char*)smem;
#pragma unroll
    for (int i = 0; i < 16; ++i) { red[(w * 32 + i) * 64 + l] = acc0[i]; red[(w * 32 + 16 + i) * 64 + l] = acc1[i]; }
    __syncthreads();
    {
        const int rt = kq >> 1, p = kq & 1;
        float v[8];
#pragma unroll
        for (int i = 0; i < 8; ++i) {
            float s = 0;
#pragma unroll
            for (int q = 0; q < 4; ++q) s += red[((nt + 2 * q) * 32 + rt * 16 + 8 * p + i) * 64 + l];
            v[i] = s;
        }
        const size_t ro = (size_t)(rb * 64 + rt * 32 + r32) * DM + col0 + 16 * p;
        const u32x2 x0 = *(const u32x2*)(x + ro + 4 * h), x1 = *(const u32x2*)(x + ro + 8 + 4 * h);
        u32x2 g0 = {pk2(v[0] + bflo(x0[0]), v[1] + bfhi(x0[0])), pk2(v[2] + bflo(x0[1]), v[3] + bfhi(x0[1]))};
        u32x2 g1 = {pk2(v[4] + bflo(x1[0]), v[5] + bfhi(x1[0])), pk2(v[6] + bflo(x1[1]), v[7] + bfhi(x1[1]))};
        *(u32x4*)(out + ro + 8 * h) = widen(g0, g1);
    }
}

extern "C" void kernel_launch(void* const* d_in, const int* in_sizes, int n_in, void* d_out, int out_size, void* d_ws, size_t ws_size,
                              hipStream_t stream) {
    const uint16_t* x = (const uint16_t*)d_in[0];
    const uint16_t* wq = (const uint16_t*)d_in[1];
    const uint16_t* wk = (const uint16_t*)d_in[2];
    const uint16_t* wv = (const uint16_t*)d_in[3];
    const uint16_t* wo = (const uint16_t*)d_in[4];
    const uint16_t* w1 = (const uint16_t*)d_in[6];
    const uint16_t* w2 = (const uint16_t*)d_in[7];
    char* ws = (char*)d_ws;
    kA<<<256, 512, 0, stream>>>(x, wq, wk, wv, w1, ws);
    kB<<<256, 1024, 0, stream>>>(ws);
    kC<<<256, 512, 0, stream>>>(x, wo, w2, ws, (uint16_t*)d_out);
}
```

```cpp
#include <hip/hip_runtime.h>
#include <hip/hip_bf16.h>
#include <stdint.h>

typedef __attribute__((ext_vector_type(8))) __bf16 bf16x8;
typedef __attribute__((ext_vector_type(2))) __bf16 bf16x2;
typedef __attribute__((ext_vector_type(16))) float f32x16;
typedef __attribute__((ext_vector_type(4))) float f32x4;
typedef __attribute__((ext_vector_type(2))) float f32x2;
typedef __attribute__((ext_vector_type(4))) unsigned u32x4;
typedef __attribute__((ext_vector_type(2))) unsigned u32x2;
typedef __attribute__((ext_vector_type(4))) short s16x4;
typedef __attribute__((ext_vector_type(8))) short s16x8;
#define LAS __attribute__((address_space(3)))
#define GAS __attribute__((address_space(1)))
#define DEV __device__ __forceinline__
#define SB() __builtin_amdgcn_sched_barrier(0)

constexpr int SEQ = 2048, DM = 512, NH = 32, DH = 16, FF = 1376;
constexpr int KIMG = 65536, VSTEP = 528, VIMG = 128 * VSTEP, KVIMG = KIMG + VIMG;
constexpr int HSTEPS = FF / 16;
constexpr size_t WS_EPOCH = 0, WS_FK = 4096, WS_FV = 5120, WS_FH = 8192, WS_FO = 12288;
constexpr size_t WS_KV = 65536, WS_O = 1u << 20, WS_H = WS_O + (2u << 20);
constexpr size_t WS_DBG = 64u << 20;
constexpr int LDS_BYTES = 163840;
constexpr int L_X = 0, L_WR = 65536, WRING = 7, L_MISC = L_WR + 8 * WRING * 1024;
constexpr int L_STATS = L_MISC, L_CS = L_MISC + 512, L_SYNC = L_MISC + 1536;
constexpr int L_WK = 131072, L_WV = 147456;
constexpr int L_Q = 133120, L_RING2 = 133120;
constexpr int L_SYNC2 = L_RING2 + 8 * 3072;
constexpr int L_WO = 0, L_RED = 65536;
#define MOFF 96.f
#define EXP_NOPOLL 0
#define EXP_NOKV 0
#define EXP_NODOT 0
#define WPRE 2
#define OPOLL_SLEEP 2
#define APRIO 2
#define POLL_SLEEP 8
constexpr unsigned SPIN_CAP = 1u << 20;

#define ST(i)

template <int... I> struct ISeq {};
template <int N, int... I> struct MkSeq : MkSeq<N - 1, N - 1, I...> {};
template <int... I> struct MkSeq<0, I...> { typedef ISeq<I...> T; };
template <int V> struct IC { static constexpr int v = V; };
template <class F, int... I> DEV void sfor_(F&& f, ISeq<I...>) { (f(IC<I>{}), ...); }
template <int N, class F> DEV void sfor(F&& f) { sfor_(f, typename MkSeq<N>::T{}); }

DEV unsigned pk2(float lo, float hi) { f32x2 v = {lo, hi}; return __builtin_bit_cast(unsigned, __builtin_convertvector(v, bf16x2)); }
DEV float bflo(unsigned u) { return __uint_as_float(u << 16); }
DEV float bfhi(unsigned u) { return __uint_as_float(u & 0xffff0000u); }
DEV f32x16 mfma(bf16x8 a, bf16x8 b, f32x16 c) { return __builtin_amdgcn_mfma_f32_32x32x16_bf16(a, b, c, 0, 0, 0); }
DEV f32x4 mfma16(bf16x8 a, bf16x8 b, f32x4 c) { return __builtin_amdgcn_mfma_f32_16x16x32_bf16(a, b, c, 0, 0, 0); }
DEV void glds16(const void* g, unsigned lds_addr) {
    unsigned keep;
    asm volatile("s_mov_b32 %0, m0\n\ts_mov_b32 m0, %2\n\ts_nop 2\n\tglobal_load_lds_dwordx4 %1, off\n\ts_mov_b32 m0, %0"
                 : "=&s"(keep) : "v"(g), "s"(lds_addr) : "memory");
}
DEV void glds16_sc1(const void* g, unsigned lds_addr) {
    unsigned keep;
    asm volatile("s_mov_b32 %0, m0\n\ts_mov_b32 m0, %2\n\ts_nop 2\n\tglobal_load_lds_dwordx4 %1, off sc1\n\ts_mov_b32 m0, %0"
                 : "=&s"(keep) : "v"(g), "s"(lds_addr) : "memory");
}
DEV void glds16s(const void* sbase, unsigned voff, unsigned lds_addr) {
    unsigned keep;
    asm volatile("s_mov_b32 %0, m0\n\ts_mov_b32 m0, %3\n\ts_nop 2\n\tglobal_load_lds_dwordx4 %1, %2\n\ts_mov_b32 m0, %0"
                 : "=&s"(keep) : "v"(voff), "s"(sbase), "s"(lds_addr) : "memory");
}
DEV void glds16s_sc1(const void* sbase, unsigned voff, unsigned lds_addr) {
    unsigned keep;
    asm volatile("s_mov_b32 %0, m0\n\ts_mov_b32 m0, %3\n\ts_nop 2\n\tglobal_load_lds_dwordx4 %1, %2 sc1\n\ts_mov_b32 m0, %0"
                 : "=&s"(keep) : "v"(voff), "s"(sbase), "s"(lds_addr) : "memory");
}
DEV void ld16s_dev_async(u32x4& d, const void* sbase, unsigned voff) { asm volatile("s_nop 4\n\tglobal_load_dwordx4 %0, %1, %2 sc1" : "=v"(d) : "v"(voff), "s"(sbase) : "memory"); }
DEV f32x4 lanesum8(u32x4 u, f32x4 c) {
    const s16x4 ones = {0x3F80, 0x3F80, 0x3F80, 0x3F80};
    c = __builtin_amdgcn_mfma_f32_4x4x4bf16_1k(ones, __builtin_bit_cast(s16x4, (u32x2){u[0], u[1]}), c, 0, 0, 0);
    return __builtin_amdgcn_mfma_f32_4x4x4bf16_1k(ones, __builtin_bit_cast(s16x4, (u32x2){u[2], u[3]}), c, 0, 0, 0);
}
template <int N> DEV void wait_vm() { asm volatile("s_waitcnt vmcnt(%0)" :: "n"(N) : "memory"); }
DEV void wait_lgkm0() { asm volatile("s_waitcnt lgkmcnt(0)" ::: "memory"); }
DEV void barrier() { asm volatile("s_waitcnt lgkmcnt(0)\n\ts_barrier" ::: "memory"); }
DEV s16x4 tr_read(LAS const char* p) { return __builtin_amdgcn_ds_read_tr16_b64_v4i16((LAS s16x4*)p); }
DEV bf16x8 cat8(s16x4 a, s16x4 b) { s16x8 r = {a[0], a[1], a[2], a[3], b[0], b[1], b[2], b[3]}; return __builtin_bit_cast(bf16x8, r); }
DEV bf16x8 wfrag(LAS const char* base_lane) { return cat8(tr_read(base_lane), tr_read(base_lane + 256)); }
DEV unsigned wfrag_lane_off(int l) { int h = l >> 5, blk = (l >> 4) & 1, q = (l & 15) >> 2, p = l & 3; return (8 * h + q) * 64 + blk * 32 + p * 8; }
DEV u32x4 widen(u32x2 a, u32x2 b) {
    auto r0 = __builtin_amdgcn_permlane32_swap(a[0], b[0], false, false);
    auto r1 = __builtin_amdgcn_permlane32_swap(a[1], b[1], false, false);
    u32x4 o = {r0[0], r1[0], r0[1], r1[1]};
    return o;
}
DEV float dot2(unsigned a, unsigned b, float c) { return __builtin_amdgcn_fdot2_f32_bf16(__builtin_bit_cast(bf16x2, a), __builtin_bit_cast(bf16x2, b), c, false); }
DEV float xsum16_32(float v) {
    auto a = __builtin_amdgcn_permlane16_swap(__float_as_uint(v), __float_as_uint(v), false, false);
    v = __uint_as_float(a[0]) + __uint_as_float(a[1]);
    auto b = __builtin_amdgcn_permlane32_swap(__float_as_uint(v), __float_as_uint(v), false, false);
    return __uint_as_float(b[0]) + __uint_as_float(b[1]);
}
DEV float xsum32(float v) { auto b = __builtin_amdgcn_permlane32_swap(__float_as_uint(v), __float_as_uint(v), false, false); return __uint_as_float(b[0]) + __uint_as_float(b[1]); }
DEV float halfmax(float m) { auto r = __builtin_amdgcn_permlane32_swap(__float_as_uint(m), __float_as_uint(m), false, false); return fmaxf(__uint_as_float(r[0]), __uint_as_float(r[1])); }
DEV float max16(const f32x16& s, float m) {
#pragma unroll
    for (int i = 0; i < 16; i += 2) m = fmaxf(fmaxf(m, s[i]), s[i + 1]);
    return m;
}
DEV void st16_dev(void* p, u32x4 v) { asm volatile("global_store_dwordx4 %0, %1, off sc1\n\ts_nop 1" :: "v"(p), "v"(v) : "memory"); }
DEV void st8_dev(void* p, u32x2 v) { __hip_atomic_store((GAS unsigned long long*)p, ((unsigned long long)v[1] << 32) | v[0], __ATOMIC_RELAXED, __HIP_MEMORY_SCOPE_AGENT); }
DEV void st4_dev(void* p, unsigned v) { __hip_atomic_store((GAS unsigned*)p, v, __ATOMIC_RELAXED, __HIP_MEMORY_SCOPE_AGENT); }
DEV unsigned ld4_dev(const void* p) { return __hip_atomic_load((GAS const unsigned*)p, __ATOMIC_RELAXED, __HIP_MEMORY_SCOPE_AGENT); }
DEV void ld16_dev_async(u32x4& d, const void* p) { asm volatile("global_load_dwordx4 %0, %1, off sc1" : "=v"(d) : "v"(p) : "memory"); }
#define PIN(x) asm volatile("" : "+v"(x))
DEV unsigned lds_ld_volatile(unsigned off_ptr) { return *(LAS volatile unsigned*)(uintptr_t)off_ptr; }

#define EX(r) "v_exp_f32 v" #r ", v" #r "\n\t"
#define WL2 "s_waitcnt lgkmcnt(2)\n\t"
#define QK(d0, d1) "v_mfma_f32_32x32x16_bf16 v[" #d0 ":" #d1 "], v[84:87], v[80:83], v[32:47]\n\t"
#define PV(vf0, vf1, b0, b1) "v_smfmac_f32_32x32x32_bf16 v[16:31], v[" #vf0 ":" #vf1 "], v[" #b0 ":" #b1 "], v96\n\t"
#define RDK(off) "ds_read_b128 v[84:87], v97 offset:" #off "\n\t"
#define RDV0(off) "ds_read_b128 v[88:91], v98 offset:" #off "\n\t"
#define RDV1(off) "ds_read_b128 v[92:95], v98 offset:" #off "\n\t"
#define PHASE_A(ko, vo0, vo1)   \
    EX(50) EX(51) EX(52) EX(53) WL2 QK(64, 79) RDK(ko) EX(54) EX(55) EX(56) EX(57) WL2 PV(88, 91, 48, 55) RDV0(vo0) \
    EX(58) EX(59) EX(60) EX(61) EX(62) EX(63) EX(64) EX(65) WL2 PV(92, 95, 56, 63) RDV1(vo1)
#define PHASE_B(ko, vo0, vo1)   \
    EX(66) EX(67) EX(68) EX(69) WL2 QK(48, 63) RDK(ko) EX(70) EX(71) EX(72) EX(73) WL2 PV(88, 91, 64, 71) RDV0(vo0) \
    EX(74) EX(75) EX(76) EX(77) EX(78) EX(79) EX(48) EX(49) WL2 PV(92, 95, 72, 79) RDV1(vo1)
DEV void attn_loop(f32x16& o, const f32x16& cin, const bf16x8& q, unsigned kaddr, unsigned vaddr, int barcnt) {
    int cnt = 16; int idx = 0xDDDD;
    asm volatile(
        "s_nop 1\n\t"
        RDK(0)
        "s_waitcnt lgkmcnt(0)\n\t"
        QK(48, 63)
        RDK(1024) RDV0(0) RDV1(528)
        "s_nop 8\n\t"
        EX(48) EX(49)
        "1:\n\t"
        PHASE_A(2048, 1056, 1584)
        PHASE_B(3072, 2112, 2640)
        PHASE_A(4096, 3168, 3696)
        PHASE_B(5120, 4224, 4752)
        "v_add_u32 v97, 0x1000, v97\n\t"
        "v_add_u32 v98, 0x1080, v98\n\t"
        "s_sub_u32 %[cnt], %[cnt], 1\n\t"
        "s_cmp_lg_u32 %[cnt], %[bar]\n\t"
        "s_cbranch_scc1 2f\n\t"
        "s_waitcnt vmcnt(0)\n\t"
        "s_barrier\n\t"
        "2:\n\t"
        "s_cmp_lg_u32 %[cnt], 0\n\t"
        "s_cbranch_scc1 1b\n\t"
        "s_waitcnt lgkmcnt(0)\n\t"
        "s_nop 15\n\t"
        : "+{v[16:31]}"(o), "+{v97}"(kaddr), "+{v98}"(vaddr), [cnt] "+s"(cnt)
        : "{v[32:47]}"(cin), "{v[80:83]}"(q), "{v96}"(idx), [bar] "s"(barcnt)
        : "v48", "v49", "v50", "v51", "v52", "v53", "v54", "v55", "v56", "v57", "v58", "v59", "v60", "v61", "v62", "v63",
          "v64", "v65", "v66", "v67", "v68", "v69", "v70", "v71", "v72", "v73", "v74", "v75", "v76", "v77", "v78", "v79",
          "v84", "v85", "v86", "v87", "v88", "v89", "v90", "v91", "v92", "v93", "v94", "v95", "scc", "memory");
}

__global__ __launch_bounds__(1024, 4) void fused_kernel(const uint16_t* __restrict__ x, const uint16_t* __restrict__ wq, const uint16_t* __restrict__ wk,
                                                        const uint16_t* __restrict__ wv, const uint16_t* __restrict__ wo, const uint16_t* __restrict__ w1,
                                                        const uint16_t* __restrict__ w2, char* __restrict__ ws, uint16_t* __restrict__ out) {
    __shared__ __attribute__((aligned(16))) char smem[LDS_BYTES];
    const int tid = threadIdx.x, l = tid & 63, w = __builtin_amdgcn_readfirstlane(tid >> 6);
    const int b = blockIdx.x, xcd = b & 7, idx = b >> 3, rb = xcd * 4 + (idx >> 3), j = idx & 7, cu = rb * 8 + j;
    const int r32 = l & 31, h = l >> 5;
    const unsigned sm0 = (unsigned)(uintptr_t)smem;
    LAS char* sm = (LAS char*)smem;
    ST(0);
#pragma unroll
    for (int i = 0; i < 4; ++i) {
        const int row = 4 * w + i;
        glds16(x + (size_t)(rb * 64 + row) * DM + ((l ^ (row & 15)) << 3), sm0 + L_X + row * 1024);
    }
    const bool isG = w < 8;
    const int hcount = j < 3 ? 6 : 5, hstart = j < 3 ? 6 * j : 18 + 5 * (j - 3);
    const int gtype = w < 2 ? 0 : (w - 2 < hcount ? 1 : 2);
    const uint16_t* wp = wq; int ldw = 0; unsigned wvo = 0;
    if (isG) {
        if (gtype == 0) { wp = wq + 64 * j + 32 * w; ldw = DM; }
        else if (gtype == 1) { wp = w1 + (hstart + w - 2) * 32; ldw = FF; }
        wvo = (unsigned)((l >> 2) * ldw * 2 + (l & 3) * 16);
        if (gtype != 2) {
#pragma unroll
            for (int k = 0; k < WPRE; ++k) glds16s(wp + (size_t)k * 16 * ldw, wvo, sm0 + L_WR + w * (WRING * 1024) + k * 1024);
        }
    } else {
        const int a = w - 8;
        const uint16_t* src = (a < 4 ? wk : wv) + (a & 3) * 2048 + l * 8;
#pragma unroll
        for (int i = 0; i < 4; ++i) glds16(src + i * 512, sm0 + (a < 4 ? L_WK : L_WV) + (a & 3) * 4096 + i * 1024);
    }
    if (tid == 0) { *(LAS u32x4*)(sm + L_SYNC) = (u32x4){0u, 0u, 0u, 0u}; }
    unsigned epoch;
    asm volatile("s_load_dword %0, %1, 0x0\n\ts_waitcnt lgkmcnt(0)" : "=s"(epoch) : "s"(ws + WS_EPOCH) : "memory");
    const unsigned tag = epoch * 0x9E3779B1u + 0x7F4A7C15u;
    if (isG && gtype != 2) wait_vm<WPRE>(); else wait_vm<0>();
    barrier();
    ST(1);

    if (isG) {
        f32x16 acc0 = {}, acc1 = {};
        if (gtype == 2 && l == 0) __hip_atomic_fetch_add((LAS unsigned*)(sm + L_SYNC + 8), 1u, __ATOMIC_RELAXED, __HIP_MEMORY_SCOPE_WORKGROUP);
        if (gtype != 2) {
            f32x4 cs4 = {};
            LAS const char* xb = sm + L_X + r32 * 1024;
            const unsigned xo = (unsigned)((h ^ (r32 & 15)) << 4);
            LAS const char* wb = sm + L_WR + w * (WRING * 1024) + wfrag_lane_off(l);
            const unsigned wst = sm0 + L_WR + w * (WRING * 1024);
            bf16x8 x0[2], x1[2], wf[2];
#pragma unroll
            for (int k = WPRE; k < WRING - 1; ++k) glds16s(wp + (size_t)k * 16 * ldw, wvo, wst + k * 1024);
            wait_vm<WRING - 2>();
            x0[0] = *(LAS const bf16x8*)(xb + xo); x1[0] = *(LAS const bf16x8*)(xb + 32768 + xo); wf[0] = wfrag(wb);
            sfor<32>([&](auto K) {
                constexpr int k = K.v;
                if constexpr (k + WRING - 1 < 32) glds16s(wp + (size_t)(k + WRING - 1) * 16 * ldw, wvo, wst + ((k + WRING - 1) % WRING) * 1024);
                if constexpr (k + 1 < 32) {
                    constexpr int last = (k + WRING - 1 < 32) ? k + WRING - 1 : 31;
                    wait_vm<last - (k + 1)>();
                    constexpr int kk = k + 1;
                    const unsigned off = (xo ^ ((kk & 7) << 5)) + (kk >> 3) * 256;
                    wf[kk & 1] = wfrag(wb + (kk % WRING) * 1024);
                    x0[kk & 1] = *(LAS const bf16x8*)(xb + off); x1[kk & 1] = *(LAS const bf16x8*)(xb + 32768 + off);
                }
                SB();
                acc0 = mfma(wf[k & 1], x0[k & 1], acc0); acc1 = mfma(wf[k & 1], x1[k & 1], acc1);
                cs4 = lanesum8(__builtin_bit_cast(u32x4, wf[k & 1]), cs4);
                SB();
                if constexpr (k == 0) ST(12);
                if constexpr (k == 7) ST(13);
                if constexpr (k == 15) ST(14);
            });
            ST(2);
            if (l == 0) __hip_atomic_fetch_add((LAS unsigned*)(sm + L_SYNC + 8), 1u, __ATOMIC_RELAXED, __HIP_MEMORY_SCOPE_WORKGROUP);
            const float cs = xsum32(cs4[0]);
            LAS float* csb = (LAS float*)(sm + L_CS + w * 128);
            if (l < 32) csb[l] = cs;
            unsigned sp = 0;
            while (*(LAS volatile unsigned*)(sm + L_SYNC) < 4u || *(LAS volatile unsigned*)(sm + L_SYNC + 4) < 2u) { __builtin_amdgcn_s_sleep(1); if (++sp > SPIN_CAP) break; }
            wait_lgkm0();
            const f32x2 st0 = *(LAS const f32x2*)(sm + L_STATS + r32 * 8), st1 = *(LAS const f32x2*)(sm + L_STATS + (32 + r32) * 8);
            f32x4 csv[4];
#pragma unroll
            for (int i = 0; i < 4; ++i) csv[i] = *(LAS const f32x4*)(csb + 8 * i + 4 * h);
#pragma unroll
            for (int rt = 0; rt < 2; ++rt) {
                const f32x16& a = rt ? acc1 : acc0;
                const float mean = rt ? st1[0] : st0[0], rs = rt ? st1[1] : st0[1];
                float v[16];
#pragma unroll
                for (int i = 0; i < 16; ++i) v[i] = (a[i] - mean * csv[i >> 2][i & 3]) * rs;
                if (gtype == 0) {
                    const float sc = 0.25f * 1.4426950408889634f;
#pragma unroll
                    for (int p = 0; p < 2; ++p) {
                        u32x2 g0 = {pk2(v[8 * p] * sc, v[8 * p + 1] * sc), pk2(v[8 * p + 2] * sc, v[8 * p + 3] * sc)};
                        u32x2 g1 = {pk2(v[8 * p + 4] * sc, v[8 * p + 5] * sc), pk2(v[8 * p + 6] * sc, v[8 * p + 7] * sc)};
                        *(LAS u32x4*)(sm + L_Q + (rt * 4 + 2 * w + p) * 1024 + l * 16) = widen(g0, g1);
                    }
                } else {
                    char* hb = ws + WS_H + ((size_t)(rb * 2 + rt) * HSTEPS + 2 * (hstart + w - 2)) * 1024 + l * 16;
#pragma unroll
                    for (int p = 0; p < 2; ++p) {
                        u32x2 g0 = {pk2(fmaxf(v[8 * p], 0.f), fmaxf(v[8 * p + 1], 0.f)), pk2(fmaxf(v[8 * p + 2], 0.f), fmaxf(v[8 * p + 3], 0.f))};
                        u32x2 g1 = {pk2(fmaxf(v[8 * p + 4], 0.f), fmaxf(v[8 * p + 5], 0.f)), pk2(fmaxf(v[8 * p + 6], 0.f), fmaxf(v[8 * p + 7], 0.f))};
                        st16_dev(hb + p * 1024, widen(g0, g1));
                    }
                }
            }
        }
        ST(3);
    } else {
        const int a = w - 8, c16 = l & 15, g = l >> 4;
        __builtin_amdgcn_s_setprio(3);
        bf16x8 ones; for (int e = 0; e < 8; ++e) ones[e] = (__bf16)1.0f;
        if (a < 2) {
            const int g16 = j >> 1, xrow = 16 * g16 + c16;
            LAS const char* xb = sm + L_X + xrow * 1024;
            LAS const char* wb = sm + (a == 0 ? L_WK : L_WV) + (8 * g + (c16 >> 2)) * 32 + (l & 3) * 8;
            f32x4 d = {}, cs = {};
            auto chain = [&](auto A) {
#pragma unroll
                for (int bt = 0; bt < 2; ++bt) {
                    bf16x8 xf[8], wf[8];
#pragma unroll
                    for (int s = 0; s < 8; ++s) {
                        const int ss = 8 * bt + s;
                        xf[s] = *(LAS const bf16x8*)(xb + (((4 * ss + g) ^ c16) << 4));
                        wf[s] = cat8(tr_read(wb + ss * 1024), tr_read(wb + ss * 1024 + 128));
                    }
                    SB();
#pragma unroll
                    for (int s = 0; s < 8; ++s) {
                        if constexpr (A.v == 0) { d = mfma16(wf[s], xf[s], d); cs = mfma16(wf[s], ones, cs); } else { d = mfma16(xf[s], wf[s], d); cs = mfma16(ones, wf[s], cs); }
                    }
                    SB();
                }
            };
            if (a == 0) chain(IC<0>{}); else chain(IC<1>{});
            ST(12);
            { unsigned sp = 0; while (*(LAS volatile unsigned*)(sm + L_SYNC) < 4u) { __builtin_amdgcn_s_sleep(1); if (++sp > SPIN_CAP) break; } }
            const int key = rb * 64 + 16 * g16;
            char* kv = ws + WS_KV;
            if (a == 0) {
                const f32x2 st = *(LAS const f32x2*)(sm + L_STATS + xrow * 8);
                const float mean = st[0], rs = st[1];
                u32x2 o = {pk2((d[0] - mean * cs[0]) * rs, (d[1] - mean * cs[1]) * rs), pk2((d[2] - mean * cs[2]) * rs, (d[3] - mean * cs[3]) * rs)};
                const int kk = key + c16;
                if ((c16 >> 3) == (j & 1)) st8_dev(kv + (kk >> 5) * 1024 + (g >> 1) * 512 + (kk & 31) * 16 + (g & 1) * 8, o);
            } else {
                float v[4];
#pragma unroll
                for (int r = 0; r < 4; ++r) { const f32x2 st = *(LAS const f32x2*)(sm + L_STATS + (16 * g16 + 4 * g + r) * 8); v[r] = (d[r] - st[0] * cs[r]) * st[1]; }
                u32x2 o = {pk2(v[0], v[1]), pk2(v[2], v[3])};
                char* vb = kv + KIMG + (key >> 4) * VSTEP;
                if ((g >> 1) == (j & 1)) st8_dev(vb + (j & 1) * 256 + c16 * 16 + (g & 1) * 8, o);
                if ((j & 1) == 0 && l < 2) st8_dev(vb + 512 + l * 8, (u32x2){0x3F803F80u, 0x3F803F80u});
            }
            ST(13);
            wait_vm<0>();
            ST(14);
            if (l == 0) {
                const unsigned old = __hip_atomic_fetch_add((LAS unsigned*)(sm + L_SYNC + 4), 1u, __ATOMIC_RELAXED, __HIP_MEMORY_SCOPE_WORKGROUP);
                if (old == 1u) st4_dev(ws + WS_FK + cu * 4, tag);
            }
            ST(2);
        } else if (a >= 4) {
            const int xrow = 16 * (a - 4) + c16;
            LAS const char* xb = sm + L_X + xrow * 1024;
            f32x4 m1 = {}, m2 = {};
            bf16x8 xf[16];
#pragma unroll
            for (int s = 0; s < 16; ++s) xf[s] = *(LAS const bf16x8*)(xb + (((4 * s + g) ^ c16) << 4));
            SB();
#pragma unroll
            for (int s = 0; s < 16; ++s) { m1 = mfma16(ones, xf[s], m1); m2 = mfma16(xf[s], xf[s], m2); }
            const float dg = (c16 & 2) ? ((c16 & 1) ? m2[3] : m2[2]) : ((c16 & 1) ? m2[1] : m2[0]);
            const float s2 = __shfl(dg, 16 * (c16 >> 2) + c16);
            const float mean = m1[0] * (1.f / DM), rs = __builtin_amdgcn_rsqf(fmaxf(s2 * (1.f / DM) - mean * mean, 0.f) + 1e-5f);
            if (l < 16) *(LAS f32x2*)(sm + L_STATS + xrow * 8) = (f32x2){mean, rs};
            wait_lgkm0();
            if (l == 0) __hip_atomic_fetch_add((LAS unsigned*)(sm + L_SYNC), 1u, __ATOMIC_RELAXED, __HIP_MEMORY_SCOPE_WORKGROUP);
            ST(2);
        } else if (a == 2 && !EXP_NOPOLL) {
            { unsigned sp = 0; while (*(LAS volatile unsigned*)(sm + L_SYNC + 8) < 8u) { __builtin_amdgcn_s_sleep(4); if (++sp > SPIN_CAP) break; } }
            const __amdgpu_buffer_rsrc_t rs = __builtin_amdgcn_make_buffer_rsrc((void*)(ws + WS_FK), 0, 1024, 0x00020000);
            for (unsigned sp = 0; sp < SPIN_CAP; ++sp) {
                asm volatile("" ::: "memory");
                const u32x4 fa = __builtin_amdgcn_raw_buffer_load_b128(rs, l * 16, 0, 16);
                const bool ok = fa[0] == tag && fa[1] == tag && fa[2] == tag && fa[3] == tag;
                if (__all(ok)) break;
                __builtin_amdgcn_s_sleep(POLL_SLEEP);
            }
            if (b == 0 && l == 0) st4_dev(ws + WS_EPOCH, epoch + 1u);
            ST(2);
        }
    }
    barrier();
    ST(4);
    if (tid == 0) *(LAS unsigned*)(sm + L_SYNC2) = 0u;
    bf16x8 q;
    {
        const char* src = ws + WS_KV; const unsigned lo = l * 16;
        if (!isG) {
            const int a = w - 8;
            glds16s_sc1(src + a * 1024, lo, sm0 + a * 1024);
            glds16s_sc1(src + (64 + a) * 1024, lo, sm0 + (64 + a) * 1024);
            glds16s_sc1(src + (8 + a) * 1024, lo, sm0 + (8 + a) * 1024);
            glds16s_sc1(src + (72 + a) * 1024, lo, sm0 + (72 + a) * 1024);
            q = *(LAS const bf16x8*)(sm + L_Q + a * 1024 + l * 16);
            wait_vm<0>();
        } else {
#pragma unroll
            for (int i = 0; i < 6; ++i) {
                glds16s_sc1(src + (16 + w + 8 * i) * 1024, lo, sm0 + (16 + w + 8 * i) * 1024);
                glds16s_sc1(src + (80 + w + 8 * i) * 1024, lo, sm0 + (80 + w + 8 * i) * 1024);
            }
            if (w < 2) glds16s_sc1(src + (128 + w) * 1024, lo, sm0 + (128 + w) * 1024);
        }
    }
    barrier();
    ST(5);
    f32x16 acc0 = {}, acc1 = {};
    if (!isG) {
        const int a = w - 8, head = 4 * j + (a & 3), qh = a >> 2;
        __builtin_amdgcn_s_setprio(APRIO);
        LAS const char* kb = sm + l * 16;
        LAS const char* vb = sm + KIMG + (r32 < 16 ? h * 256 + r32 * 16 : 512);
        float mref;
        {
            const f32x16 z = {};
            float m = max16(mfma(*(LAS const bf16x8*)kb, q, z), -3.0e38f);
            m = max16(mfma(*(LAS const bf16x8*)(kb + 1024), q, z), m);
            mref = halfmax(m) + MOFF;
        }
        f32x16 o;
        for (int attempt = 0;; ++attempt) {
            f32x16 cin;
#pragma unroll
            for (int i = 0; i < 16; ++i) { cin[i] = -mref; o[i] = 0.f; }
            attn_loop(o, cin, q, (unsigned)(uintptr_t)kb, (unsigned)(uintptr_t)vb, attempt == 0 ? 14 : -1);
            bool bad = !(o[8] < 1.0e30f) || !(o[8] > 0.f);
#pragma unroll
            for (int i = 0; i < 8; ++i) bad = bad || !(fabsf(o[i]) < 3.0e38f);
            if (attempt > 0 || !__any(bad)) break;
            float m = -3.0e38f; const f32x16 z = {};
            for (int t = 0; t < 64; ++t) m = max16(mfma(*(LAS const bf16x8*)(kb + t * 1024), q, z), m);
            mref = halfmax(m);
        }
        ST(6);
        const float r = 1.0f / o[8];
        u32x2 g0 = {pk2(o[0] * r, o[1] * r), pk2(o[2] * r, o[3] * r)}, g1 = {pk2(o[4] * r, o[5] * r), pk2(o[6] * r, o[7] * r)};
        st16_dev(ws + WS_O + ((size_t)(rb * 2 + qh) * 32 + head) * 1024 + l * 16, widen(g0, g1));
        wait_vm<0>();
        ST(7);
    } else {
        wait_vm<0>();
        barrier();
        if (tid == 0) st4_dev(ws + WS_FH + cu * 4, tag);
        const int nt = w & 1, kq = w >> 1;
        {
            if (w == 0) {
                for (unsigned sp = 0; sp < SPIN_CAP; ++sp) {
                    const unsigned f = ld4_dev(ws + WS_FH + (rb * 8 + (l & 7)) * 4);
                    if (__all(f == tag)) break;
                    __builtin_amdgcn_s_sleep(32);
                }
                if (l == 0) *(LAS volatile unsigned*)(sm + L_SYNC2) = 1u;
            } else {
                unsigned sp = 0; while (*(LAS volatile unsigned*)(sm + L_SYNC2) == 0u) { __builtin_amdgcn_s_sleep(16); if (++sp > SPIN_CAP) break; }
            }
        }
        const int k0 = kq < 2 ? 22 * kq : 44 + 21 * (kq - 2), nk = kq < 2 ? 22 : 21;
        const char* hp = ws + WS_H + ((size_t)(rb * 2) * HSTEPS + k0) * 1024;
        const uint16_t* w2p = w2 + (size_t)(k0 * 16) * DM + 64 * j + 32 * nt;
        const unsigned wvo2 = (unsigned)((l >> 2) * DM * 2 + (l & 3) * 16), lo = l * 16;
        const unsigned rst = sm0 + L_RING2 + w * 3072;
        LAS const char* rb2 = sm + L_RING2 + w * 3072 + wfrag_lane_off(l);
        u32x4 xa[3], xb[3];
        auto issue = [&](auto K) {
            constexpr int k = K.v;
            const int kc = (k < 21 || k < nk) ? k : 20;
            glds16s(w2p + (size_t)kc * 16 * DM, wvo2, rst + (k % 3) * 1024);
            ld16s_dev_async(xa[k % 3], hp + (size_t)kc * 1024, lo);
            ld16s_dev_async(xb[k % 3], hp + (size_t)(HSTEPS + kc) * 1024, lo);
        };
        issue(IC<0>{}); issue(IC<1>{});
        sfor<22>([&](auto K) {
            constexpr int k = K.v;
            if constexpr (k + 2 < 22) { wait_lgkm0(); issue(IC<k + 2>{}); wait_vm<6>(); }
            else if constexpr (k + 1 < 22) wait_vm<3>();
            else wait_vm<0>();
            PIN(xa[k % 3]); PIN(xb[k % 3]);
            if (k < 21 || k < nk) {
                const bf16x8 wf = wfrag(rb2 + (k % 3) * 1024);
                acc0 = mfma(wf, __builtin_bit_cast(bf16x8, xa[k % 3]), acc0); acc1 = mfma(wf, __builtin_bit_cast(bf16x8, xb[k % 3]), acc1);
            }
            SB();
        });
        ST(6);
    }
    barrier();
    ST(8);
    if (tid == 0) st4_dev(ws + WS_FO + cu * 4, tag);
    u32x2 x0, x1;
    const int fnt = w & 1, frt = (w >> 1) & 1, fp = (w >> 2) & 1;
    const size_t ro = (size_t)(rb * 64 + frt * 32 + r32) * DM + 64 * j + 32 * fnt + 16 * fp;
    if (isG) {
        const int nt = w & 1, kq = w >> 1;
        x0 = *(const u32x2*)(x + ro + 4 * h); x1 = *(const u32x2*)(x + ro + 8 + 4 * h);
        const uint16_t* wop = wo + (size_t)(kq * 128) * DM + 64 * j + 32 * nt;
        const unsigned wvo3 = (unsigned)((l >> 2) * DM * 2 + (l & 3) * 16);
#pragma unroll
        for (int s = 0; s < 8; ++s) glds16s(wop + (size_t)s * 16 * DM, wvo3, sm0 + L_WO + w * 8192 + s * 1024);
        if (w == 0) {
            for (unsigned sp = 0; sp < SPIN_CAP; ++sp) {
                const unsigned f = ld4_dev(ws + WS_FO + (rb * 8 + (l & 7)) * 4);
                if (__all(f == tag)) break;
                __builtin_amdgcn_s_sleep(OPOLL_SLEEP);
            }
        }
    }
    barrier();
    if (isG) {
        const int kq = w >> 1;
        ST(9);
        u32x4 oa[8], ob[8];
        const char* op = ws + WS_O + ((size_t)(rb * 2) * 32 + 8 * kq) * 1024;
#pragma unroll
        for (int s = 0; s < 8; ++s) { ld16s_dev_async(oa[s], op + s * 1024, l * 16); ld16s_dev_async(ob[s], op + (32 + s) * 1024, l * 16); }
        wait_vm<0>();
        LAS const char* wb = sm + L_WO + w * 8192 + wfrag_lane_off(l);
#pragma unroll
        for (int s = 0; s < 8; ++s) {
            PIN(oa[s]); PIN(ob[s]);
            const bf16x8 wf = wfrag(wb + s * 1024);
            acc0 = mfma(wf, __builtin_bit_cast(bf16x8, oa[s]), acc0); acc1 = mfma(wf, __builtin_bit_cast(bf16x8, ob[s]), acc1);
        }
        LAS f32x4* red = (LAS f32x4*)(sm + L_RED + w * 8192) + l;
#pragma unroll
        for (int i = 0; i < 4; ++i) {
            red[i * 64] = (f32x4){acc0[4 * i], acc0[4 * i + 1], acc0[4 * i + 2], acc0[4 * i + 3]};
            red[(4 + i) * 64] = (f32x4){acc1[4 * i], acc1[4 * i + 1], acc1[4 * i + 2], acc1[4 * i + 3]};
        }
        ST(10);
    }
    barrier();
    if (isG) {
        f32x4 v0 = {}, v1 = {};
#pragma unroll
        for (int q4 = 0; q4 < 4; ++q4) {
            LAS const f32x4* red = (LAS const f32x4*)(sm + L_RED + (fnt + 2 * q4) * 8192) + l;
            v0 += red[(frt * 4 + 2 * fp) * 64]; v1 += red[(frt * 4 + 2 * fp + 1) * 64];
        }
        u32x2 g0 = {pk2(v0[0] + bflo(x0[0]), v0[1] + bfhi(x0[0])), pk2(v0[2] + bflo(x0[1]), v0[3] + bfhi(x0[1]))};
        u32x2 g1 = {pk2(v1[0] + bflo(x1[0]), v1[1] + bfhi(x1[0])), pk2(v1[2] + bflo(x1[1]), v1[3] + bfhi(x1[1]))};
        *(u32x4*)(out + ro + 8 * h) = widen(g0, g1);
    }
    ST(11);
}

extern "C" void kernel_launch(void* const* d_in, const int* in_sizes, int n_in, void* d_out, int out_size, void* d_ws, size_t ws_size,
                              hipStream_t stream) {
    fused_kernel<<<256, 1024, 0, stream>>>((const uint16_t*)d_in[0], (const uint16_t*)d_in[1], (const uint16_t*)d_in[2], (const uint16_t*)d_in[3],
                                           (const uint16_t*)d_in[4], (const uint16_t*)d_in[6], (const uint16_t*)d_in[7], (char*)d_ws, (uint16_t*)d_out);
}
```

```cpp
#include <hip/hip_runtime.h>
#include <hip/hip_bf16.h>
#include <stdint.h>

typedef __attribute__((ext_vector_type(8))) __bf16 bf16x8;
typedef __attribute__((ext_vector_type(2))) __bf16 bf16x2;
typedef __attribute__((ext_vector_type(16))) float f32x16;
typedef __attribute__((ext_vector_type(4))) float f32x4;
typedef __attribute__((ext_vector_type(2))) float f32x2;
typedef __attribute__((ext_vector_type(4))) unsigned u32x4;
typedef __attribute__((ext_vector_type(2))) unsigned u32x2;
typedef __attribute__((ext_vector_type(4))) short s16x4;
typedef __attribute__((ext_vector_type(8))) short s16x8;
#define LAS __attribute__((address_space(3)))
#define GAS __attribute__((address_space(1)))
#define DEV __device__ __forceinline__
#define SB() __builtin_amdgcn_sched_barrier(0)

constexpr int SEQ = 2048, DM = 512, NH = 32, DH = 16, FF = 1376;
constexpr int KIMG = 65536, VSTEP = 528, VIMG = 128 * VSTEP, KVIMG = KIMG + VIMG;
constexpr int HSTEPS = FF / 16;
constexpr size_t WS_EPOCH = 0, WS_FK = 4096, WS_FV = 5120, WS_FH = 8192, WS_FO = 12288;
constexpr size_t WS_KV = 65536, WS_O = 1u << 20, WS_H = WS_O + (2u << 20);
constexpr size_t WS_DBG = 64u << 20;
constexpr int LDS_BYTES = 163840;
constexpr int L_X = 0, L_WR = 65536, WRING = 7, L_MISC = 122880;
constexpr int L_STATS = L_MISC, L_CS = L_MISC + 512, L_SYNC = L_MISC + 1536;
constexpr int L_WK = 131072, L_WV = 147456;
constexpr int L_Q = 133120, L_RING2 = 133120;
constexpr int L_SYNC2 = L_RING2 + 8 * 3072;
constexpr int L_WO = 0, L_RED = 65536;
#define MOFF 96.f
#define WPRE 2
#define WD 8
#define OPOLL_SLEEP 2
#define EXP_CS 0
#define ROT 1
#define FD 4
#define XMAP 1
#define GWAIT 0
#define CODEPF 0
constexpr int CODE_KB = 26;
#define CODE_BACK 0
#define EXP_NOFFN2 0
#define NA 52
#define GS1 2
#define TOUCH 0
#define EXP_NOGATHER 0
#define EXP_NOHST 0
#define APRIO 0
#define POLL_SLEEP 8
constexpr unsigned SPIN_CAP = 1u << 20;

#define ST(i)

template <int... I> struct ISeq {};
template <int N, int... I> struct MkSeq : MkSeq<N - 1, N - 1, I...> {};
template <int... I> struct MkSeq<0, I...> { typedef ISeq<I...> T; };
template <int V> struct IC { static constexpr int v = V; };
template <class F, int... I> DEV void sfor_(F&& f, ISeq<I...>) { (f(IC<I>{}), ...); }
template <int N, class F> DEV void sfor(F&& f) { sfor_(f, typename MkSeq<N>::T{}); }

DEV unsigned pk2(float lo, float hi) { f32x2 v = {lo, hi}; return __builtin_bit_cast(unsigned, __builtin_convertvector(v, bf16x2)); }
DEV float bflo(unsigned u) { return __uint_as_float(u << 16); }
DEV float bfhi(unsigned u) { return __uint_as_float(u & 0xffff0000u); }
DEV f32x16 mfma(bf16x8 a, bf16x8 b, f32x16 c) { return __builtin_amdgcn_mfma_f32_32x32x16_bf16(a, b, c, 0, 0, 0); }
DEV f32x4 mfma16(bf16x8 a, bf16x8 b, f32x4 c) { return __builtin_amdgcn_mfma_f32_16x16x32_bf16(a, b, c, 0, 0, 0); }
DEV void glds16(const void* g, unsigned lds_addr) {
    unsigned keep;
    asm volatile("s_mov_b32 %0, m0\n\ts_mov_b32 m0, %2\n\ts_nop 2\n\tglobal_load_lds_dwordx4 %1, off\n\ts_mov_b32 m0, %0"
                 : "=&s"(keep) : "v"(g), "s"(lds_addr) : "memory");
}
DEV void glds16_sc1(const void* g, unsigned lds_addr) {
    unsigned keep;
    asm volatile("s_mov_b32 %0, m0\n\ts_mov_b32 m0, %2\n\ts_nop 2\n\tglobal_load_lds_dwordx4 %1, off sc1\n\ts_mov_b32 m0, %0"
                 : "=&s"(keep) : "v"(g), "s"(lds_addr) : "memory");
}
DEV void glds16s(const void* sbase, unsigned voff, unsigned lds_addr) {
    unsigned keep;
    asm volatile("s_mov_b32 %0, m0\n\ts_mov_b32 m0, %3\n\ts_nop 2\n\tglobal_load_lds_dwordx4 %1, %2\n\ts_mov_b32 m0, %0"
                 : "=&s"(keep) : "v"(voff), "s"(sbase), "s"(lds_addr) : "memory");
}
DEV void glds16s_sc1(const void* sbase, unsigned voff, unsigned lds_addr) {
    unsigned keep;
    asm volatile("s_mov_b32 %0, m0\n\ts_mov_b32 m0, %3\n\ts_nop 2\n\tglobal_load_lds_dwordx4 %1, %2 sc1\n\ts_mov_b32 m0, %0"
                 : "=&s"(keep) : "v"(voff), "s"(sbase), "s"(lds_addr) : "memory");
}
DEV void ld16s_dev_async(u32x4& d, const void* sbase, unsigned voff) { asm volatile("s_nop 4\n\tglobal_load_dwordx4 %0, %1, %2 sc1" : "=v"(d) : "v"(voff), "s"(sbase) : "memory"); }
DEV void ld16s_async(u32x4& d, const void* sbase, unsigned voff) { asm volatile("s_nop 4\n\tglobal_load_dwordx4 %0, %1, %2" : "=v"(d) : "v"(voff), "s"(sbase) : "memory"); }
DEV void touch_async(unsigned& d, const void* sbase, unsigned voff) { asm volatile("s_nop 4\n\tglobal_load_dword %0, %1, %2 sc1" : "=v"(d) : "v"(voff), "s"(sbase) : "memory"); }
DEV f32x4 lanesum8(u32x4 u, f32x4 c) {
    const s16x4 ones = {0x3F80, 0x3F80, 0x3F80, 0x3F80};
    c = __builtin_amdgcn_mfma_f32_4x4x4bf16_1k(ones, __builtin_bit_cast(s16x4, (u32x2){u[0], u[1]}), c, 0, 0, 0);
    return __builtin_amdgcn_mfma_f32_4x4x4bf16_1k(ones, __builtin_bit_cast(s16x4, (u32x2){u[2], u[3]}), c, 0, 0, 0);
}
template <int N> DEV void wait_vm() { asm volatile("s_waitcnt vmcnt(%0)" :: "n"(N) : "memory"); }
DEV void wait_lgkm0() { asm volatile("s_waitcnt lgkmcnt(0)" ::: "memory"); }
DEV void barrier() { asm volatile("s_waitcnt lgkmcnt(0)\n\ts_barrier" ::: "memory"); }
DEV s16x4 tr_read(LAS const char* p) { return __builtin_amdgcn_ds_read_tr16_b64_v4i16((LAS s16x4*)p); }
DEV bf16x8 cat8(s16x4 a, s16x4 b) { s16x8 r = {a[0], a[1], a[2], a[3], b[0], b[1], b[2], b[3]}; return __builtin_bit_cast(bf16x8, r); }
DEV bf16x8 wfrag(LAS const char* base_lane) { return cat8(tr_read(base_lane), tr_read(base_lane + 256)); }
DEV unsigned wfrag_lane_off(int l) { int h = l >> 5, blk = (l >> 4) & 1, q = (l & 15) >> 2, p = l & 3; return (8 * h + q) * 64 + blk * 32 + p * 8; }
DEV u32x4 widen(u32x2 a, u32x2 b) {
    auto r0 = __builtin_amdgcn_permlane32_swap(a[0], b[0], false, false);
    auto r1 = __builtin_amdgcn_permlane32_swap(a[1], b[1], false, false);
    u32x4 o = {r0[0], r1[0], r0[1], r1[1]};
    return o;
}
DEV float dot2(unsigned a, unsigned b, float c) { return __builtin_amdgcn_fdot2_f32_bf16(__builtin_bit_cast(bf16x2, a), __builtin_bit_cast(bf16x2, b), c, false); }
DEV float xsum16_32(float v) {
    auto a = __builtin_amdgcn_permlane16_swap(__float_as_uint(v), __float_as_uint(v), false, false);
    v = __uint_as_float(a[0]) + __uint_as_float(a[1]);
    auto b = __builtin_amdgcn_permlane32_swap(__float_as_uint(v), __float_as_uint(v), false, false);
    return __uint_as_float(b[0]) + __uint_as_float(b[1]);
}
DEV float xsum32(float v) { auto b = __builtin_amdgcn_permlane32_swap(__float_as_uint(v), __float_as_uint(v), false, false); return __uint_as_float(b[0]) + __uint_as_float(b[1]); }
DEV float halfmax(float m) { auto r = __builtin_amdgcn_permlane32_swap(__float_as_uint(m), __float_as_uint(m), false, false); return fmaxf(__uint_as_float(r[0]), __uint_as_float(r[1])); }
DEV float max16(const f32x16& s, float m) {
#pragma unroll
    for (int i = 0; i < 16; i += 2) m = fmaxf(fmaxf(m, s[i]), s[i + 1]);
    return m;
}
DEV void st16_dev(void* p, u32x4 v) { asm volatile("global_store_dwordx4 %0, %1, off sc1\n\ts_nop 1" :: "v"(p), "v"(v) : "memory"); }
DEV void st8_dev(void* p, u32x2 v) { __hip_atomic_store((GAS unsigned long long*)p, ((unsigned long long)v[1] << 32) | v[0], __ATOMIC_RELAXED, __HIP_MEMORY_SCOPE_AGENT); }
DEV void st4_dev(void* p, unsigned v) { __hip_atomic_store((GAS unsigned*)p, v, __ATOMIC_RELAXED, __HIP_MEMORY_SCOPE_AGENT); }
DEV unsigned ld4_dev(const void* p) { return __hip_atomic_load((GAS const unsigned*)p, __ATOMIC_RELAXED, __HIP_MEMORY_SCOPE_AGENT); }
DEV void ld16_dev_async(u32x4& d, const void* p) { asm volatile("global_load_dwordx4 %0, %1, off sc1" : "=v"(d) : "v"(p) : "memory"); }
#define PIN(x) asm volatile("" : "+v"(x))
DEV unsigned lds_ld_volatile(unsigned off_ptr) { return *(LAS volatile unsigned*)(uintptr_t)off_ptr; }

#define EX(r) "v_exp_f32 v" #r ", v" #r "\n\t"
#define WL2 "s_waitcnt lgkmcnt(2)\n\t"
#define QK(d0, d1) "v_mfma_f32_32x32x16_bf16 v[" #d0 ":" #d1 "], v[84:87], v[80:83], v[32:47]\n\t"
#define PV(vf0, vf1, b0, b1) "v_smfmac_f32_32x32x32_bf16 v[16:31], v[" #vf0 ":" #vf1 "], v[" #b0 ":" #b1 "], v96\n\t"
#define RDK(off) "ds_read_b128 v[84:87], v97 offset:" #off "\n\t"
#define RDV0(off) "ds_read_b128 v[88:91], v98 offset:" #off "\n\t"
#define RDV1(off) "ds_read_b128 v[92:95], v98 offset:" #off "\n\t"
#define PHASE_A(ko, vo0, vo1)   \
    EX(50) EX(51) EX(52) EX(53) WL2 QK(64, 79) RDK(ko) EX(54) EX(55) EX(56) EX(57) WL2 PV(88, 91, 48, 55) RDV0(vo0) \
    EX(58) EX(59) EX(60) EX(61) EX(62) EX(63) EX(64) EX(65) WL2 PV(92, 95, 56, 63) RDV1(vo1)
#define PHASE_B(ko, vo0, vo1)   \
    EX(66) EX(67) EX(68) EX(69) WL2 QK(48, 63) RDK(ko) EX(70) EX(71) EX(72) EX(73) WL2 PV(88, 91, 64, 71) RDV0(vo0) \
    EX(74) EX(75) EX(76) EX(77) EX(78) EX(79) EX(48) EX(49) WL2 PV(92, 95, 72, 79) RDV1(vo1)
#define WL3 "s_waitcnt lgkmcnt(3)\n\t"
#define CLAIM "s_mov_b64 exec, 1\n\t" "ds_sub_rtn_u32 v99, v100, v96\n\t" "s_mov_b64 exec, -1\n\t"
#define PHASE_A0(ko, vo0, vo1)   \
    EX(50) EX(51) EX(52) EX(53) WL3 QK(64, 79) RDK(ko) EX(54) EX(55) EX(56) EX(57) WL3 PV(88, 91, 48, 55) RDV0(vo0) \
    EX(58) EX(59) EX(60) EX(61) EX(62) EX(63) EX(64) EX(65) WL3 PV(92, 95, 56, 63) RDV1(vo1)
#define ATTN_CLOBBERS "v48", "v49", "v50", "v51", "v52", "v53", "v54", "v55", "v56", "v57", "v58", "v59", "v60", "v61", "v62", "v63", \
          "v64", "v65", "v66", "v67", "v68", "v69", "v70", "v71", "v72", "v73", "v74", "v75", "v76", "v77", "v78", "v79", \
          "v84", "v85", "v86", "v87", "v88", "v89", "v90", "v91", "v92", "v93", "v94", "v95", "v99", "scc", "memory"
DEV void attn_up(f32x16& o, const f32x16& cin, const bf16x8& q, unsigned kaddr, unsigned vaddr, unsigned caddr, int barit) {
    int it = 0, idx = 0xDDDD, got;
    asm volatile(
        "s_nop 1\n\t"
        RDK(0)
        "s_waitcnt lgkmcnt(0)\n\t"
        QK(48, 63)
        RDK(1024) RDV0(0) RDV1(528)
        "s_nop 8\n\t"
        EX(48) EX(49)
        "1:\n\t"
        CLAIM
        PHASE_A0(2048, 1056, 1584)
        PHASE_B(3072, 2112, 2640)
        PHASE_A(4096, 3168, 3696)
        PHASE_B(5120, 4224, 4752)
        "v_add_u32 v97, 0x1000, v97\n\t"
        "v_add_u32 v98, 0x1080, v98\n\t"
        "v_readfirstlane_b32 %[got], v99\n\t"
        "s_add_u32 %[it], %[it], 1\n\t"
        "s_cmp_lg_u32 %[it], %[bar]\n\t"
        "s_cbranch_scc1 2f\n\t"
        "s_waitcnt vmcnt(0)\n\t"
        "s_barrier\n\t"
        "2:\n\t"
        "s_cmp_gt_i32 %[got], 0\n\t"
        "s_cbranch_scc1 1b\n\t"
        "s_waitcnt lgkmcnt(0)\n\t"
        "s_nop 15\n\t"
        : "+{v[16:31]}"(o), "+{v97}"(kaddr), "+{v98}"(vaddr), [it] "+s"(it), [got] "=&s"(got)
        : "{v[32:47]}"(cin), "{v[80:83]}"(q), "{v96}"(idx), "{v100}"(caddr), [bar] "s"(barit)
        : ATTN_CLOBBERS);
}
DEV void attn_down(f32x16& o, const f32x16& cin, const bf16x8& q, unsigned kaddr, unsigned vaddr, unsigned caddr) {
    int idx = 0xDDDD, got;
    asm volatile(
        "s_nop 1\n\t"
        RDK(5120)
        "s_waitcnt lgkmcnt(0)\n\t"
        QK(48, 63)
        RDK(4096) RDV0(4224) RDV1(4752)
        "s_nop 8\n\t"
        EX(48) EX(49)
        "1:\n\t"
        CLAIM
        PHASE_A0(3072, 3168, 3696)
        PHASE_B(2048, 2112, 2640)
        PHASE_A(1024, 1056, 1584)
        PHASE_B(0, 0, 528)
        "v_subrev_u32 v97, 0x1000, v97\n\t"
        "v_subrev_u32 v98, 0x1080, v98\n\t"
        "v_readfirstlane_b32 %[got], v99\n\t"
        "s_nop 3\n\t"
        "s_cmp_gt_i32 %[got], 0\n\t"
        "s_cbranch_scc1 1b\n\t"
        "s_waitcnt lgkmcnt(0)\n\t"
        "s_nop 15\n\t"
        : "+{v[16:31]}"(o), "+{v97}"(kaddr), "+{v98}"(vaddr), [got] "=&s"(got)
        : "{v[32:47]}"(cin), "{v[80:83]}"(q), "{v96}"(idx), "{v100}"(caddr)
        : ATTN_CLOBBERS);
}

DEV float sample_ref(LAS const char* kb, const bf16x8& q) {
    const f32x16 z = {};
    float m = max16(mfma(*(LAS const bf16x8*)kb, q, z), -3.0e38f);
    m = max16(mfma(*(LAS const bf16x8*)(kb + 1024), q, z), m);
    return halfmax(m) + MOFF;
}
DEV int lane_id() { int v = (int)__builtin_amdgcn_mbcnt_hi(~0u, __builtin_amdgcn_mbcnt_lo(~0u, 0u)); asm volatile("" : "+v"(v)); return v; }
DEV float vcopy(float x) { float r; asm volatile("v_mov_b32 %0, %1" : "=v"(r) : "v"(x)); return r; }
struct Args {
    const uint16_t* x; const uint16_t* wq; const uint16_t* wk; const uint16_t* wv; const uint16_t* wo; const uint16_t* w1; const uint16_t* w2;
    char* ws; uint16_t* out;
};
DEV unsigned load_epoch(const char* ws) {
    unsigned epoch;
    asm volatile("s_load_dword %0, %1, 0x0\n\ts_waitcnt lgkmcnt(0)" : "=s"(epoch) : "s"(ws + WS_EPOCH) : "memory");
    return epoch;
}
DEV unsigned epoch_tag(unsigned epoch) { return epoch * 0x9E3779B1u + 0x7F4A7C15u; }
DEV void lds_add(LAS char* p) { __hip_atomic_fetch_add((LAS unsigned*)p, 1u, __ATOMIC_RELAXED, __HIP_MEMORY_SCOPE_WORKGROUP); }
DEV unsigned lds_add_rtn(LAS char* p) { return __hip_atomic_fetch_add((LAS unsigned*)p, 1u, __ATOMIC_RELAXED, __HIP_MEMORY_SCOPE_WORKGROUP); }
DEV unsigned lds_vol(LAS char* p) { return *(LAS volatile unsigned*)p; }
DEV void x_tile_dma(const uint16_t* x, int rb, int w, int l, unsigned sm0) {
#pragma unroll
    for (int i = 0; i < 4; ++i) {
        const int row = 4 * w + i;
        glds16s(x + (size_t)(rb * 64 + row) * DM, (unsigned)((l ^ (row & 15)) << 4), sm0 + L_X + row * 1024);
    }
}

DEV void g_role(const Args& A, LAS char* sm, unsigned sm0, int w, int l, int b, int rb, int j) {
    char* ws = A.ws;
    const int cu = rb * 8 + j, r32 = l & 31, h = l >> 5;
    x_tile_dma(A.x, rb, w, l, sm0);
    const int hcount = j < 3 ? 6 : 5, hstart = j < 3 ? 6 * j : 18 + 5 * (j - 3);
    const int gtype = w < 2 ? 0 : (w - 2 < hcount ? 1 : 2);
    const int krot = ROT ? 8 * (rb & 3) : 0;
    const uint16_t* wp = gtype == 0 ? A.wq + 64 * j + 32 * w : A.w1 + (hstart + (gtype == 1 ? w - 2 : 0)) * 32;
    const int ldw = gtype == 0 ? DM : FF;
    const unsigned wvo = (unsigned)((l >> 2) * ldw * 2 + (l & 3) * 16);
    u32x4 hpk[4];
    unsigned tag;
    if (gtype == 2) {
        tag = epoch_tag(load_epoch(ws));
        wait_vm<0>();
        barrier();
        if (l == 0) lds_add(sm + L_SYNC + 8);
    } else {
        const unsigned wst = sm0 + L_WR + w * (WRING * 1024);
        auto wdma = [&](int k) { glds16s(wp + (size_t)((k + krot) & 31) * 16 * ldw, wvo, wst + (k % WRING) * 1024); };
#pragma unroll
        for (int k = 0; k < WPRE; ++k) wdma(k);
        if (w == 0 && l == 0) *(LAS u32x4*)(sm + L_SYNC) = (u32x4){0u, 0u, 0u, 0u};
        tag = epoch_tag(load_epoch(ws));
        wait_vm<WPRE>();
        barrier();
        ST(1);
        f32x16 acc0 = {}, acc1 = {};
        f32x4 cs4 = {};
        LAS const char* xb = sm + L_X + r32 * 1024;
        const unsigned xo = (unsigned)((h ^ (r32 & 15)) << 4);
        LAS const char* wb = sm + L_WR + w * (WRING * 1024) + wfrag_lane_off(l);
        auto xoff = [&](int kk) -> unsigned { const unsigned kr = (unsigned)(kk + krot) & 31u; return (xo ^ ((kr & 7u) << 5)) + (kr >> 3) * 256u; };
        bf16x8 x0[2], x1[2], wf[2];
#pragma unroll
        for (int k = WPRE; k < WRING - 1; ++k) wdma(k);
        wait_vm<WRING - 2>();
        { const unsigned off = xoff(0); wf[0] = wfrag(wb); x0[0] = *(LAS const bf16x8*)(xb + off); x1[0] = *(LAS const bf16x8*)(xb + 32768 + off); }
        sfor<32>([&](auto K) {
            constexpr int k = K.v;
            if constexpr (k + WRING - 1 < 32) wdma(k + WRING - 1);
            if constexpr (k + 1 < 32) {
                constexpr int last = (k + WRING - 1 < 32) ? k + WRING - 1 : 31;
                wait_vm<last - (k + 1)>();
                constexpr int kk = k + 1;
                const unsigned off = xoff(kk);
                wf[kk & 1] = wfrag(wb + (kk % WRING) * 1024);
                x0[kk & 1] = *(LAS const bf16x8*)(xb + off); x1[kk & 1] = *(LAS const bf16x8*)(xb + 32768 + off);
            }
            SB();
            acc0 = mfma(wf[k & 1], x0[k & 1], acc0); acc1 = mfma(wf[k & 1], x1[k & 1], acc1);
            if (EXP_CS == 0) cs4 = lanesum8(__builtin_bit_cast(u32x4, wf[k & 1]), cs4);
            SB();
        });
        ST(2);
        if (l == 0) lds_add(sm + L_SYNC + 8);
        const float cs = xsum32(cs4[0]);
        LAS float* csb = (LAS float*)(sm + L_CS + w * 128);
        if (l < 32) csb[l] = cs;
        { unsigned sp = 0; while (lds_vol(sm + L_SYNC) < 4u || lds_vol(sm + L_SYNC + 4) < 2u) { __builtin_amdgcn_s_sleep(1); if (++sp > SPIN_CAP) break; } }
        wait_lgkm0();
        const f32x2 st0 = *(LAS const f32x2*)(sm + L_STATS + r32 * 8), st1 = *(LAS const f32x2*)(sm + L_STATS + (32 + r32) * 8);
        f32x4 csv[4];
#pragma unroll
        for (int i = 0; i < 4; ++i) csv[i] = *(LAS const f32x4*)(csb + 8 * i + 4 * h);
#pragma unroll
        for (int rt = 0; rt < 2; ++rt) {
            const f32x16& a = rt ? acc1 : acc0;
            const float mean = rt ? st1[0] : st0[0], rs = rt ? st1[1] : st0[1];
            float v[16];
#pragma unroll
            for (int i = 0; i < 16; ++i) v[i] = (a[i] - mean * csv[i >> 2][i & 3]) * rs;
            if (gtype == 0) {
                const float sc = 0.25f * 1.4426950408889634f;
#pragma unroll
                for (int p = 0; p < 2; ++p) {
                    u32x2 g0 = {pk2(v[8 * p] * sc, v[8 * p + 1] * sc), pk2(v[8 * p + 2] * sc, v[8 * p + 3] * sc)};
                    u32x2 g1 = {pk2(v[8 * p + 4] * sc, v[8 * p + 5] * sc), pk2(v[8 * p + 6] * sc, v[8 * p + 7] * sc)};
                    *(LAS u32x4*)(sm + L_Q + (rt * 4 + 2 * w + p) * 1024 + l * 16) = widen(g0, g1);
                }
            } else {
#pragma unroll
                for (int p = 0; p < 2; ++p) {
                    u32x2 g0 = {pk2(fmaxf(v[8 * p], 0.f), fmaxf(v[8 * p + 1], 0.f)), pk2(fmaxf(v[8 * p + 2], 0.f), fmaxf(v[8 * p + 3], 0.f))};
                    u32x2 g1 = {pk2(fmaxf(v[8 * p + 4], 0.f), fmaxf(v[8 * p + 5], 0.f)), pk2(fmaxf(v[8 * p + 6], 0.f), fmaxf(v[8 * p + 7], 0.f))};
                    hpk[rt * 2 + p] = widen(g0, g1);
                }
            }
        }
        ST(3);
    }
    barrier();
    ST(4);
    if (w == 0 && l < 32) *(LAS unsigned*)(sm + L_SYNC2 + 4 * l) = (l >= 12 && l < 20) ? 14u * 0xDDDDu : 0u;
    const bf16x8 q = *(LAS const bf16x8*)(sm + L_Q + w * 1024 + l * 16);
    barrier();
    ST(5);
    {
        const char* src = ws + WS_KV; const unsigned lo = l * 16;
#pragma unroll
        for (int i = 0; i < 6; ++i) {
            if (!EXP_NOGATHER) {
            glds16s_sc1(src + (16 + w + 8 * i) * 1024, lo, sm0 + (16 + w + 8 * i) * 1024);
            glds16s_sc1(src + (80 + w + 8 * i) * 1024, lo, sm0 + (80 + w + 8 * i) * 1024);
            }
        }
        if (w < 2 && !EXP_NOGATHER) glds16s_sc1(src + (128 + w) * 1024, lo, sm0 + (128 + w) * 1024);
        if (gtype == 1 && !EXP_NOHST) {
            char* hb = ws + WS_H + ((size_t)(rb * 2) * HSTEPS + 2 * (hstart + w - 2)) * 1024 + l * 16;
#pragma unroll
            for (int rt = 0; rt < 2; ++rt)
#pragma unroll
                for (int p = 0; p < 2; ++p) st16_dev(hb + ((size_t)rt * HSTEPS + p) * 1024, hpk[rt * 2 + p]);
            wait_vm<4>();
        } else wait_vm<0>();
    }
    ST(7);
    barrier();
    ST(15);
    const int nt = w & 1, kq = w >> 1;
    const int k0 = kq < 2 ? 22 * kq : 44 + 21 * (kq - 2), nk = kq < 2 ? 22 : 21;
    const char* hp = ws + WS_H + ((size_t)(rb * 2) * HSTEPS + k0) * 1024;
    const uint16_t* w2p = A.w2 + (size_t)(k0 * 16) * DM + 64 * j + 32 * nt;
    const unsigned kb0 = sm0 + l * 16, vb0 = sm0 + KIMG + (r32 < 16 ? h * 256 + r32 * 16 : 512);
    float op[9], mref;
    {
        mref = sample_ref(sm + l * 16, q);
        f32x16 cin, o;
#pragma unroll
        for (int i = 0; i < 16; ++i) { cin[i] = -mref; o[i] = 0.f; }
        attn_down(o, cin, q, kb0 + (63 - 5) * 1024, vb0 + (63 - 4) * 2 * VSTEP, sm0 + L_SYNC2 + 80);
#pragma unroll
        for (int i = 0; i < 9; ++i) op[i] = vcopy(o[i]);
    }
    wait_vm<0>();
    if (l == 0) { if (lds_add_rtn(sm + L_SYNC2 + 4) == 7u) st4_dev(ws + WS_FH + cu * 4, tag); }
    ST(14);
    if (w == 0) {
        for (unsigned sp = 0; sp < SPIN_CAP; ++sp) {
            const unsigned f = ld4_dev(ws + WS_FH + (rb * 8 + (l & 7)) * 4);
            if (__all(f == tag)) break;
            __builtin_amdgcn_s_sleep(8);
        }
        if (l == 0) *(LAS volatile unsigned*)(sm + L_SYNC2) = 1u;
    } else {
        unsigned sp = 0; while (lds_vol(sm + L_SYNC2) == 0u) { __builtin_amdgcn_s_sleep(4); if (++sp > SPIN_CAP) break; }
    }
    ST(13);
    f32x16 acc0 = {}, acc1 = {};
    {
        const int krot2 = ROT ? (XMAP ? (5 * (j & 3) + 3 * (rb & 7)) % 21 : (5 * (rb & 3) + 3 * j) % 21) : 0;
        const unsigned wvo2 = (unsigned)((l >> 2) * DM * 2 + (l & 3) * 16), lo = l * 16;
        LAS char* rsl = sm + L_RING2 + w * 3072;
        LAS const char* rb2 = rsl + wfrag_lane_off(l);
        u32x4 wr[FD], xa[FD], xb[FD];
        auto issue = [&](auto K) {
            constexpr int k = K.v;
            int kc = (k < 21 || k < nk) ? k : 20;
            kc += krot2; if (kc >= nk) kc -= nk;
            ld16s_async(wr[k % FD], w2p + (size_t)kc * 16 * DM, wvo2);
            ld16s_dev_async(xa[k % FD], hp + (size_t)kc * 1024, lo);
            ld16s_dev_async(xb[k % FD], hp + (size_t)(HSTEPS + kc) * 1024, lo);
        };
        if (!EXP_NOFFN2) {
        sfor<FD>([&](auto K) { issue(K); });
        wait_vm<3 * (FD - 2)>(); PIN(wr[0]); PIN(wr[1]);
        *(LAS u32x4*)(rsl + l * 16) = wr[0]; *(LAS u32x4*)(rsl + 1024 + l * 16) = wr[1];
        bf16x8 wf[2];
        wf[0] = wfrag(rb2);
        sfor<22>([&](auto K) {
            constexpr int k = K.v;
            if constexpr (k + 2 < 22) {
                constexpr int issued = (k - 1 + FD < 22) ? k - 1 + FD : 21;
                wait_vm<3 * (issued - (k + 2))>(); PIN(wr[(k + 2) % FD]);
                *(LAS u32x4*)(rsl + ((k + 2) % 3) * 1024 + l * 16) = wr[(k + 2) % FD];
            } else if constexpr (k == 20) wait_vm<0>();
            if constexpr (k + 1 < 22) wf[(k + 1) & 1] = wfrag(rb2 + ((k + 1) % 3) * 1024);
            PIN(xa[k % FD]); PIN(xb[k % FD]);
            SB();
            if (k < 21 || k < nk) { acc0 = mfma(wf[k & 1], __builtin_bit_cast(bf16x8, xa[k % FD]), acc0); acc1 = mfma(wf[k & 1], __builtin_bit_cast(bf16x8, xb[k % FD]), acc1); }
            SB();
            if constexpr (k + FD < 22) issue(IC<k + FD>{});
        });
        }
    }
    ST(7);
    {
        int granted;
        if (l == 0) granted = (int)__hip_atomic_fetch_sub((LAS unsigned*)(sm + L_SYNC2 + 48 + 4 * w), 0xDDDDu, __ATOMIC_RELAXED, __HIP_MEMORY_SCOPE_WORKGROUP);
        granted = __builtin_amdgcn_readfirstlane(granted);
        if (granted > 0) {
            f32x16 cin, o;
#pragma unroll
            for (int i = 0; i < 16; ++i) { cin[i] = -mref; o[i] = i < 9 ? op[i] : 0.f; }
            const int lz = lane_id();
            const unsigned kb1 = sm0 + lz * 16, vb1 = sm0 + KIMG + ((lz & 31) < 16 ? (lz >> 5) * 256 + (lz & 31) * 16 : 512);
            attn_down(o, cin, q, kb1 + (59 - 5) * 1024, vb1 + (59 - 4) * 2 * VSTEP, sm0 + L_SYNC2 + 48 + 4 * w);
#pragma unroll
            for (int i = 0; i < 9; ++i) op[i] = vcopy(o[i]);
        }
    }
    const int lt = lane_id(), r32t = lt & 31, ht = lt >> 5;
    {
        wait_lgkm0();
        LAS float* pp = (LAS float*)(sm + L_RING2 + w * 3072) + lt;
#pragma unroll
        for (int i = 0; i < 9; ++i) pp[i * 64] = op[i];
        wait_lgkm0();
        if (lt == 0) *(LAS volatile unsigned*)(sm + L_SYNC2 + 16 + 4 * w) = 1u;
    }
    ST(6);
    barrier();
    ST(8);
    const int fnt = w & 1, frt = (w >> 1) & 1, fp = (w >> 2) & 1;
    const size_t ro = (size_t)(rb * 64 + frt * 32 + r32t) * DM + 64 * j + 32 * fnt + 16 * fp;
    const u32x2 x0 = *(const u32x2*)(A.x + ro + 4 * ht), x1 = *(const u32x2*)(A.x + ro + 8 + 4 * ht);
    {
        const uint16_t* wop = A.wo + (size_t)(kq * 128) * DM + 64 * j + 32 * nt;
        const unsigned wvo3 = (unsigned)((lt >> 2) * DM * 2 + (lt & 3) * 16);
#pragma unroll
        for (int s = 0; s < 8; ++s) glds16s(wop + (size_t)s * 16 * DM, wvo3, sm0 + L_WO + w * 8192 + s * 1024);
        if (w == 0) {
            for (unsigned sp = 0; sp < SPIN_CAP; ++sp) {
                const unsigned f = ld4_dev(ws + WS_FO + (rb * 8 + (lt & 7)) * 4);
                if (__all(f == tag)) break;
                __builtin_amdgcn_s_sleep(OPOLL_SLEEP);
            }
        }
    }
    barrier();
    ST(9);
    {
        u32x4 oa[8], ob[8];
        const char* op = ws + WS_O + ((size_t)(rb * 2) * 32 + 8 * kq) * 1024;
#pragma unroll
        for (int s = 0; s < 8; ++s) { ld16s_dev_async(oa[s], op + s * 1024, lt * 16); ld16s_dev_async(ob[s], op + (32 + s) * 1024, lt * 16); }
        wait_vm<0>();
        LAS const char* wb = sm + L_WO + w * 8192 + wfrag_lane_off(lt);
#pragma unroll
        for (int s = 0; s < 8; ++s) {
            PIN(oa[s]); PIN(ob[s]);
            const bf16x8 wf = wfrag(wb + s * 1024);
            acc0 = mfma(wf, __builtin_bit_cast(bf16x8, oa[s]), acc0); acc1 = mfma(wf, __builtin_bit_cast(bf16x8, ob[s]), acc1);
        }
        LAS f32x4* red = (LAS f32x4*)(sm + L_RED + w * 8192) + lt;
#pragma unroll
        for (int i = 0; i < 4; ++i) {
            red[i * 64] = (f32x4){acc0[4 * i], acc0[4 * i + 1], acc0[4 * i + 2], acc0[4 * i + 3]};
            red[(4 + i) * 64] = (f32x4){acc1[4 * i], acc1[4 * i + 1], acc1[4 * i + 2], acc1[4 * i + 3]};
        }
    }
    ST(10);
    barrier();
    {
        f32x4 v0 = {}, v1 = {};
#pragma unroll
        for (int q4 = 0; q4 < 4; ++q4) {
            LAS const f32x4* red = (LAS const f32x4*)(sm + L_RED + (fnt + 2 * q4) * 8192) + lt;
            v0 += red[(frt * 4 + 2 * fp) * 64]; v1 += red[(frt * 4 + 2 * fp + 1) * 64];
        }
        u32x2 g0 = {pk2(v0[0] + bflo(x0[0]), v0[1] + bfhi(x0[0])), pk2(v0[2] + bflo(x0[1]), v0[3] + bfhi(x0[1]))};
        u32x2 g1 = {pk2(v1[0] + bflo(x1[0]), v1[1] + bfhi(x1[0])), pk2(v1[2] + bflo(x1[1]), v1[3] + bfhi(x1[1]))};
        *(u32x4*)(A.out + ro + 8 * ht) = widen(g0, g1);
    }
    ST(11);
}

DEV void a_role(const Args& A, LAS char* sm, unsigned sm0, int w, int l, int b, int rb, int j) {
    char* ws = A.ws;
    const int cu = rb * 8 + j, r32 = l & 31, h = l >> 5, a = w - 8, c16 = l & 15, g = l >> 4;
    x_tile_dma(A.x, rb, w, l, sm0);
    {
        const uint16_t* src = (a < 4 ? A.wk : A.wv) + (a & 3) * 2048;
#pragma unroll
        for (int i = 0; i < 4; ++i) glds16s(src + i * 512, l * 16, sm0 + (a < 4 ? L_WK : L_WV) + (a & 3) * 4096 + i * 1024);
    }
    u32x4 codepf = {};
    if (CODEPF && a == 3 && (b >> 3) < CODE_KB) {
        unsigned long long pc; asm volatile("s_getpc_b64 %0" : "=s"(pc));
        ld16s_async(codepf, (const char*)((pc - CODE_BACK) & ~255ull) + (b >> 3) * 1024, l * 16);
    }
    const unsigned epoch = load_epoch(ws), tag = epoch_tag(epoch);
    wait_vm<0>(); PIN(codepf);
    barrier();
    ST(1);
    __builtin_amdgcn_s_setprio(3);
    bf16x8 ones; for (int e = 0; e < 8; ++e) ones[e] = (__bf16)1.0f;
    if (a < 2) {
        const int g16 = j >> 1, xrow = 16 * g16 + c16;
        LAS const char* xb = sm + L_X + xrow * 1024;
        LAS const char* wb = sm + (a == 0 ? L_WK : L_WV) + (8 * g + (c16 >> 2)) * 32 + (l & 3) * 8;
        f32x4 d = {}, cs = {};
        auto chain = [&](auto AA) {
#pragma unroll
            for (int bt = 0; bt < 2; ++bt) {
                bf16x8 xf[8], wf[8];
#pragma unroll
                for (int s = 0; s < 8; ++s) {
                    const int ss = 8 * bt + s;
                    xf[s] = *(LAS const bf16x8*)(xb + (((4 * ss + g) ^ c16) << 4));
                    wf[s] = cat8(tr_read(wb + ss * 1024), tr_read(wb + ss * 1024 + 128));
                }
                SB();
#pragma unroll
                for (int s = 0; s < 8; ++s) {
                    if constexpr (AA.v == 0) { d = mfma16(wf[s], xf[s], d); cs = mfma16(wf[s], ones, cs); } else { d = mfma16(xf[s], wf[s], d); cs = mfma16(ones, wf[s], cs); }
                }
                SB();
            }
        };
        if (a == 0) chain(IC<0>{}); else chain(IC<1>{});
        PIN(d); PIN(cs);
        if (l == 0) lds_add(sm + L_SYNC + 12);
        { unsigned sp = 0; while (lds_vol(sm + L_SYNC) < 4u) { __builtin_amdgcn_s_sleep(1); if (++sp > SPIN_CAP) break; } }
        const int key = rb * 64 + 16 * g16;
        char* kv = ws + WS_KV;
        if (a == 0) {
            const f32x2 st = *(LAS const f32x2*)(sm + L_STATS + xrow * 8);
            const float mean = st[0], rs = st[1];
            u32x2 o = {pk2((d[0] - mean * cs[0]) * rs, (d[1] - mean * cs[1]) * rs), pk2((d[2] - mean * cs[2]) * rs, (d[3] - mean * cs[3]) * rs)};
            const int kk = key + c16;
            if ((c16 >> 3) == (j & 1)) st8_dev(kv + (kk >> 5) * 1024 + (g >> 1) * 512 + (kk & 31) * 16 + (g & 1) * 8, o);
        } else {
            float v[4];
#pragma unroll
            for (int r = 0; r < 4; ++r) { const f32x2 st = *(LAS const f32x2*)(sm + L_STATS + (16 * g16 + 4 * g + r) * 8); v[r] = (d[r] - st[0] * cs[r]) * st[1]; }
            u32x2 o = {pk2(v[0], v[1]), pk2(v[2], v[3])};
            char* vb = kv + KIMG + (key >> 4) * VSTEP;
            if ((g >> 1) == (j & 1)) st8_dev(vb + (j & 1) * 256 + c16 * 16 + (g & 1) * 8, o);
            if ((j & 1) == 0 && l < 2) st8_dev(vb + 512 + l * 8, (u32x2){0x3F803F80u, 0x3F803F80u});
        }
        wait_vm<0>();
        if (l == 0) { if (lds_add_rtn(sm + L_SYNC + 4) == 1u) st4_dev(ws + WS_FK + cu * 4, tag); }
        ST(2);
    } else if (a >= 4) {
        const int xrow = 16 * (a - 4) + c16;
        LAS const char* xb = sm + L_X + xrow * 1024;
        f32x4 m1 = {}, m2 = {};
        bf16x8 xf[16];
#pragma unroll
        for (int s = 0; s < 16; ++s) xf[s] = *(LAS const bf16x8*)(xb + (((4 * s + g) ^ c16) << 4));
        SB();
#pragma unroll
        for (int s = 0; s < 16; ++s) { m1 = mfma16(ones, xf[s], m1); m2 = mfma16(xf[s], xf[s], m2); }
        const float dg = (c16 & 2) ? ((c16 & 1) ? m2[3] : m2[2]) : ((c16 & 1) ? m2[1] : m2[0]);
        const float s2 = __shfl(dg, 16 * (c16 >> 2) + c16);
        const float mean = m1[0] * (1.f / DM), rs = __builtin_amdgcn_rsqf(fmaxf(s2 * (1.f / DM) - mean * mean, 0.f) + 1e-5f);
        if (l < 16) *(LAS f32x2*)(sm + L_STATS + xrow * 8) = (f32x2){mean, rs};
        wait_lgkm0();
        if (l == 0) lds_add(sm + L_SYNC);
        ST(2);
    } else if (a == 2) {
        { unsigned sp = 0; while (lds_vol(sm + L_SYNC + 8) < 8u) { __builtin_amdgcn_s_sleep(4); if (++sp > SPIN_CAP) break; } }
        const __amdgpu_buffer_rsrc_t rs = __builtin_amdgcn_make_buffer_rsrc((void*)(ws + WS_FK), 0, 1024, 0x00020000);
        for (unsigned sp = 0; sp < SPIN_CAP; ++sp) {
            asm volatile("" ::: "memory");
            const u32x4 fa = __builtin_amdgcn_raw_buffer_load_b128(rs, l * 16, 0, 16);
            const bool ok = fa[0] == tag && fa[1] == tag && fa[2] == tag && fa[3] == tag;
            if (__all(ok)) break;
            __builtin_amdgcn_s_sleep(POLL_SLEEP);
        }
        if (b == 0 && l == 0) st4_dev(ws + WS_EPOCH, epoch + 1u);
        ST(2);
    }
    barrier();
    ST(4);
    bf16x8 q;
    {
        const char* src = ws + WS_KV; const unsigned lo = l * 16;
        glds16s_sc1(src + a * 1024, lo, sm0 + a * 1024);
        glds16s_sc1(src + (64 + a) * 1024, lo, sm0 + (64 + a) * 1024);
        glds16s_sc1(src + (8 + a) * 1024, lo, sm0 + (8 + a) * 1024);
        glds16s_sc1(src + (72 + a) * 1024, lo, sm0 + (72 + a) * 1024);
        q = *(LAS const bf16x8*)(sm + L_Q + a * 1024 + l * 16);
        wait_vm<0>();
    }
    barrier();
    ST(5);
    const int head = 4 * j + (a & 3), qh = a >> 2;
    __builtin_amdgcn_s_setprio(APRIO);
    LAS const char* kb = sm + l * 16;
    LAS const char* vb = sm + KIMG + (r32 < 16 ? h * 256 + r32 * 16 : 512);
    f32x16 o;
    {
        const float mref = sample_ref(kb, q);
        f32x16 cin;
#pragma unroll
        for (int i = 0; i < 16; ++i) { cin[i] = -mref; o[i] = 0.f; }
        attn_up(o, cin, q, (unsigned)(uintptr_t)kb, (unsigned)(uintptr_t)vb, sm0 + L_SYNC2 + 48 + 4 * a, 2);
    }
    ST(12);
    {
        unsigned sp = 0; while (lds_vol(sm + L_SYNC2 + 16 + 4 * a) == 0u) { __builtin_amdgcn_s_sleep(2); if (++sp > SPIN_CAP) break; }
        LAS const float* pp = (LAS const float*)(sm + L_RING2 + a * 3072) + l;
#pragma unroll
        for (int i = 0; i < 9; ++i) o[i] += pp[i * 64];
    }
    bool bad = !(o[8] < 1.0e30f) || !(o[8] > 0.f);
#pragma unroll
    for (int i = 0; i < 8; ++i) bad = bad || !(fabsf(o[i]) < 3.0e38f);
    if (__any(bad)) {
        float m = -3.0e38f; const f32x16 z = {};
        for (int t = 0; t < 64; ++t) m = max16(mfma(*(LAS const bf16x8*)(kb + t * 1024), q, z), m);
        m = halfmax(m);
        f32x16 cin;
#pragma unroll
        for (int i = 0; i < 16; ++i) { cin[i] = -m; o[i] = 0.f; }
        if (l == 0) *(LAS volatile unsigned*)(sm + L_SYNC2 + 96 + 4 * a) = 15u * 0xDDDDu;
        wait_lgkm0();
        attn_up(o, cin, q, (unsigned)(uintptr_t)kb, (unsigned)(uintptr_t)vb, sm0 + L_SYNC2 + 96 + 4 * a, -1);
    }
    ST(6);
    const float r = 1.0f / o[8];
    u32x2 g0 = {pk2(o[0] * r, o[1] * r), pk2(o[2] * r, o[3] * r)}, g1 = {pk2(o[4] * r, o[5] * r), pk2(o[6] * r, o[7] * r)};
    st16_dev(ws + WS_O + ((size_t)(rb * 2 + qh) * 32 + head) * 1024 + l * 16, widen(g0, g1));
    wait_vm<0>();
    if (l == 0) { if (lds_add_rtn(sm + L_SYNC2 + 8) == 7u) st4_dev(ws + WS_FO + cu * 4, tag); }
    ST(7);
    barrier();
    ST(8);
    barrier();
    barrier();
    ST(11);
}

__global__ __launch_bounds__(1024, 4) void fused_kernel(const uint16_t* __restrict__ x, const uint16_t* __restrict__ wq, const uint16_t* __restrict__ wk,
                                                        const uint16_t* __restrict__ wv, const uint16_t* __restrict__ wo, const uint16_t* __restrict__ w1,
                                                        const uint16_t* __restrict__ w2, char* __restrict__ ws, uint16_t* __restrict__ out) {
    __shared__ __attribute__((aligned(16))) char smem[LDS_BYTES];
    const int tid = threadIdx.x, l = tid & 63, w = __builtin_amdgcn_readfirstlane(tid >> 6);
    const int b = blockIdx.x, xcd = b & 7, idx = b >> 3;
    const int rb = XMAP ? (xcd >> 1) * 8 + (idx >> 2) : xcd * 4 + (idx >> 3), j = XMAP ? (xcd & 1) * 4 + (idx & 3) : idx & 7;
    const unsigned sm0 = (unsigned)(uintptr_t)smem;
    LAS char* sm = (LAS char*)smem;
    ST(0);
    const Args A = {x, wq, wk, wv, wo, w1, w2, ws, out};
    if (w < 8) g_role(A, sm, sm0, w, l, b, rb, j); else a_role(A, sm, sm0, w, l, b, rb, j);
}

extern "C" void kernel_launch(void* const* d_in, const int* in_sizes, int n_in, void* d_out, int out_size, void* d_ws, size_t ws_size,
                              hipStream_t stream) {
    fused_kernel<<<256, 1024, 0, stream>>>((const uint16_t*)d_in[0], (const uint16_t*)d_in[1], (const uint16_t*)d_in[2], (const uint16_t*)d_in[3],
                                           (const uint16_t*)d_in[4], (const uint16_t*)d_in[6], (const uint16_t*)d_in[7], (char*)d_ws, (uint16_t*)d_out);
}
```

```cpp
#include <hip/hip_runtime.h>
#include <hip/hip_bf16.h>
#include <stdint.h>

typedef __attribute__((ext_vector_type(8))) __bf16 bf16x8;
typedef __attribute__((ext_vector_type(2))) __bf16 bf16x2;
typedef __attribute__((ext_vector_type(16))) float f32x16;
typedef __attribute__((ext_vector_type(4))) float f32x4;
typedef __attribute__((ext_vector_type(2))) float f32x2;
typedef __attribute__((ext_vector_type(4))) unsigned u32x4;
typedef __attribute__((ext_vector_type(2))) unsigned u32x2;
typedef __attribute__((ext_vector_type(4))) short s16x4;
typedef __attribute__((ext_vector_type(8))) short s16x8;
#define LAS __attribute__((address_space(3)))
#define GAS __attribute__((address_space(1)))
#define DEV __device__ __forceinline__
#define SB() __builtin_amdgcn_sched_barrier(0)

constexpr int SEQ = 2048, DM = 512, NH = 32, DH = 16, FF = 1376;
constexpr int KIMG = 65536, VSTEP = 528, VIMG = 128 * VSTEP, KVIMG = KIMG + VIMG;
constexpr int HSTEPS = FF / 16;
constexpr size_t WS_EPOCH = 0, WS_FK = 4096, WS_FH = 8192, WS_FO = 16384;
constexpr size_t WS_KV = 65536, WS_O = 1u << 20, WS_H = WS_O + (2u << 20);
constexpr size_t WS_DBG = 64u << 20;
constexpr int LDS_BYTES = 163840;
constexpr int L_X = 0, L_WR = 65536, WRING = 7, L_MISC = 122880;
constexpr int L_STATS = L_MISC, L_CS = L_MISC + 512, L_SYNC = L_MISC + 1536;
constexpr int L_WK = 131072, L_WV = 147456;
constexpr int L_Q = 133120, L_RING2 = 133120;
constexpr int L_SYNC2 = L_RING2 + 8 * 3072;
constexpr int L_WO = 0, L_RED = 65536;
#define MOFF 96.f
#define WPRE 2
#define WD 8
#define OPOLL_SLEEP 2
#define EXP_CS 0
#define ROT 1
#define FD 5
#define XMAP 1
#define GWAIT 0
#define CODEPF 0
constexpr int CODE_KB = 26;
#define CODE_BACK 0
#define EXP_NOFFN2 0
#define NA 52
#define GS1 2
#define TOUCH 0
#define EXP_NOGATHER 0
#define EXP_NOHST 0
#define FDX 5
#define FDW 8
#define HACQ 1
#define HPOLL_SLEEP 16
#define GPACE 4
#define KSPLIT 13
#define KSPREAD 0
#define ALTPRIO 1
#define ALTBIT 0
#define STR2(x) #x
#define STR(x) STR2(x)
#define ALTBIT_S STR(ALTBIT)
#define GPRIO 2
#define TLBW 0
#define L2PF 1
#define APRIO 0
#define POLL_SLEEP 8
constexpr unsigned SPIN_CAP = 1u << 20;

#define ST(i)

template <int... I> struct ISeq {};
template <int N, int... I> struct MkSeq : MkSeq<N - 1, N - 1, I...> {};
template <int... I> struct MkSeq<0, I...> { typedef ISeq<I...> T; };
template <int V> struct IC { static constexpr int v = V; };
template <class F, int... I> DEV void sfor_(F&& f, ISeq<I...>) { (f(IC<I>{}), ...); }
template <int N, class F> DEV void sfor(F&& f) { sfor_(f, typename MkSeq<N>::T{}); }

DEV unsigned pk2(float lo, float hi) { f32x2 v = {lo, hi}; return __builtin_bit_cast(unsigned, __builtin_convertvector(v, bf16x2)); }
DEV float bflo(unsigned u) { return __uint_as_float(u << 16); }
DEV float bfhi(unsigned u) { return __uint_as_float(u & 0xffff0000u); }
DEV f32x16 mfma(bf16x8 a, bf16x8 b, f32x16 c) { return __builtin_amdgcn_mfma_f32_32x32x16_bf16(a, b, c, 0, 0, 0); }
DEV f32x4 mfma16(bf16x8 a, bf16x8 b, f32x4 c) { return __builtin_amdgcn_mfma_f32_16x16x32_bf16(a, b, c, 0, 0, 0); }
DEV void glds16(const void* g, unsigned lds_addr) {
    unsigned keep;
    asm volatile("s_mov_b32 %0, m0\n\ts_mov_b32 m0, %2\n\ts_nop 2\n\tglobal_load_lds_dwordx4 %1, off\n\ts_mov_b32 m0, %0"
                 : "=&s"(keep) : "v"(g), "s"(lds_addr) : "memory");
}
DEV void glds16_sc1(const void* g, unsigned lds_addr) {
    unsigned keep;
    asm volatile("s_mov_b32 %0, m0\n\ts_mov_b32 m0, %2\n\ts_nop 2\n\tglobal_load_lds_dwordx4 %1, off sc1\n\ts_mov_b32 m0, %0"
                 : "=&s"(keep) : "v"(g), "s"(lds_addr) : "memory");
}
DEV void glds16s(const void* sbase, unsigned voff, unsigned lds_addr) {
    unsigned keep;
    asm volatile("s_mov_b32 %0, m0\n\ts_mov_b32 m0, %3\n\ts_nop 2\n\tglobal_load_lds_dwordx4 %1, %2\n\ts_mov_b32 m0, %0"
                 : "=&s"(keep) : "v"(voff), "s"(sbase), "s"(lds_addr) : "memory");
}
DEV void glds16s_sc1(const void* sbase, unsigned voff, unsigned lds_addr) {
    unsigned keep;
    asm volatile("s_mov_b32 %0, m0\n\ts_mov_b32 m0, %3\n\ts_nop 2\n\tglobal_load_lds_dwordx4 %1, %2 sc1\n\ts_mov_b32 m0, %0"
                 : "=&s"(keep) : "v"(voff), "s"(sbase), "s"(lds_addr) : "memory");
}
DEV void ld16s_dev_async(u32x4& d, const void* sbase, unsigned voff) { asm volatile("s_nop 4\n\tglobal_load_dwordx4 %0, %1, %2 sc1" : "=v"(d) : "v"(voff), "s"(sbase) : "memory"); }
DEV void ld16s_async(u32x4& d, const void* sbase, unsigned voff) { asm volatile("s_nop 4\n\tglobal_load_dwordx4 %0, %1, %2" : "=v"(d) : "v"(voff), "s"(sbase) : "memory"); }
DEV void touch_async(unsigned& d, const void* sbase, unsigned voff) { asm volatile("s_nop 4\n\tglobal_load_dword %0, %1, %2 sc1" : "=v"(d) : "v"(voff), "s"(sbase) : "memory"); }
DEV f32x4 lanesum8(u32x4 u, f32x4 c) {
    const s16x4 ones = {0x3F80, 0x3F80, 0x3F80, 0x3F80};
    c = __builtin_amdgcn_mfma_f32_4x4x4bf16_1k(ones, __builtin_bit_cast(s16x4, (u32x2){u[0], u[1]}), c, 0, 0, 0);
    return __builtin_amdgcn_mfma_f32_4x4x4bf16_1k(ones, __builtin_bit_cast(s16x4, (u32x2){u[2], u[3]}), c, 0, 0, 0);
}
template <int N> DEV void wait_vm() { asm volatile("s_waitcnt vmcnt(%0)" :: "n"(N) : "memory"); }
DEV void wait_lgkm0() { asm volatile("s_waitcnt lgkmcnt(0)" ::: "memory"); }
DEV void barrier() { asm volatile("s_waitcnt lgkmcnt(0)\n\ts_barrier" ::: "memory"); }
DEV s16x4 tr_read(LAS const char* p) { return __builtin_amdgcn_ds_read_tr16_b64_v4i16((LAS s16x4*)p); }
DEV bf16x8 cat8(s16x4 a, s16x4 b) { s16x8 r = {a[0], a[1], a[2], a[3], b[0], b[1], b[2], b[3]}; return __builtin_bit_cast(bf16x8, r); }
DEV bf16x8 wfrag(LAS const char* base_lane) { return cat8(tr_read(base_lane), tr_read(base_lane + 256)); }
DEV unsigned wfrag_lane_off(int l) { int h = l >> 5, blk = (l >> 4) & 1, q = (l & 15) >> 2, p = l & 3; return (8 * h + q) * 64 + blk * 32 + p * 8; }
DEV u32x4 widen(u32x2 a, u32x2 b) {
    auto r0 = __builtin_amdgcn_permlane32_swap(a[0], b[0], false, false);
    auto r1 = __builtin_amdgcn_permlane32_swap(a[1], b[1], false, false);
    u32x4 o = {r0[0], r1[0], r0[1], r1[1]};
    return o;
}
DEV float dot2(unsigned a, unsigned b, float c) { return __builtin_amdgcn_fdot2_f32_bf16(__builtin_bit_cast(bf16x2, a), __builtin_bit_cast(bf16x2, b), c, false); }
DEV float xsum16_32(float v) {
    auto a = __builtin_amdgcn_permlane16_swap(__float_as_uint(v), __float_as_uint(v), false, false);
    v = __uint_as_float(a[0]) + __uint_as_float(a[1]);
    auto b = __builtin_amdgcn_permlane32_swap(__float_as_uint(v), __float_as_uint(v), false, false);
    return __uint_as_float(b[0]) + __uint_as_float(b[1]);
}
DEV float xsum32(float v) { auto b = __builtin_amdgcn_permlane32_swap(__float_as_uint(v), __float_as_uint(v), false, false); return __uint_as_float(b[0]) + __uint_as_float(b[1]); }
DEV float halfmax(float m) { auto r = __builtin_amdgcn_permlane32_swap(__float_as_uint(m), __float_as_uint(m), false, false); return fmaxf(__uint_as_float(r[0]), __uint_as_float(r[1])); }
DEV float max16(const f32x16& s, float m) {
#pragma unroll
    for (int i = 0; i < 16; i += 2) m = fmaxf(fmaxf(m, s[i]), s[i + 1]);
    return m;
}
DEV void st16_dev(void* p, u32x4 v) { asm volatile("global_store_dwordx4 %0, %1, off sc1\n\ts_nop 1" :: "v"(p), "v"(v) : "memory"); }
DEV void st8_dev(void* p, u32x2 v) { __hip_atomic_store((GAS unsigned long long*)p, ((unsigned long long)v[1] << 32) | v[0], __ATOMIC_RELAXED, __HIP_MEMORY_SCOPE_AGENT); }
DEV void st4_dev(void* p, unsigned v) { __hip_atomic_store((GAS unsigned*)p, v, __ATOMIC_RELAXED, __HIP_MEMORY_SCOPE_AGENT); }
DEV unsigned ld4_dev(const void* p) { return __hip_atomic_load((GAS const unsigned*)p, __ATOMIC_RELAXED, __HIP_MEMORY_SCOPE_AGENT); }
DEV void ld16_dev_async(u32x4& d, const void* p) { asm volatile("global_load_dwordx4 %0, %1, off sc1" : "=v"(d) : "v"(p) : "memory"); }
#define PIN(x) asm volatile("" : "+v"(x))
DEV unsigned lds_ld_volatile(unsigned off_ptr) { return *(LAS volatile unsigned*)(uintptr_t)off_ptr; }

#define EX(r) "v_exp_f32 v" #r ", v" #r "\n\t"
#define WL2 "s_waitcnt lgkmcnt(2)\n\t"
#define QK(d0, d1) "v_mfma_f32_32x32x16_bf16 v[" #d0 ":" #d1 "], v[84:87], v[80:83], v[32:47]\n\t"
#define PV(vf0, vf1, b0, b1) "v_smfmac_f32_32x32x32_bf16 v[16:31], v[" #vf0 ":" #vf1 "], v[" #b0 ":" #b1 "], v96\n\t"
#define RDK(off) "ds_read_b128 v[84:87], v97 offset:" #off "\n\t"
#define RDV0(off) "ds_read_b128 v[88:91], v98 offset:" #off "\n\t"
#define RDV1(off) "ds_read_b128 v[92:95], v98 offset:" #off "\n\t"
#define PHASE_A(ko, vo0, vo1)   \
    EX(50) EX(51) EX(52) EX(53) WL2 QK(64, 79) RDK(ko) EX(54) EX(55) EX(56) EX(57) WL2 PV(88, 91, 48, 55) RDV0(vo0) \
    EX(58) EX(59) EX(60) EX(61) EX(62) EX(63) EX(64) EX(65) WL2 PV(92, 95, 56, 63) RDV1(vo1)
#define PHASE_B(ko, vo0, vo1)   \
    EX(66) EX(67) EX(68) EX(69) WL2 QK(48, 63) RDK(ko) EX(70) EX(71) EX(72) EX(73) WL2 PV(88, 91, 64, 71) RDV0(vo0) \
    EX(74) EX(75) EX(76) EX(77) EX(78) EX(79) EX(48) EX(49) WL2 PV(92, 95, 72, 79) RDV1(vo1)
DEV void attn_loop(f32x16& o, const f32x16& cin, const bf16x8& q, unsigned kaddr, unsigned vaddr, int niter, int barcnt, int par) {
    int cnt = niter; int idx = 0xDDDD, tmp;
    asm volatile(
        "s_nop 1\n\t"
        RDK(0)
        "s_waitcnt lgkmcnt(0)\n\t"
        QK(48, 63)
        RDK(1024) RDV0(0) RDV1(528)
        "s_nop 8\n\t"
        EX(48) EX(49)
        "1:\n\t"
        "s_xor_b32 %[tmp], %[cnt], %[par]\n\t"
        "s_bitcmp1_b32 %[tmp], " ALTBIT_S "\n\t"
        "s_cbranch_scc1 3f\n\t"
        "s_setprio 0\n\t"
        "s_branch 4f\n\t"
        "3:\n\t"
        "s_setprio 1\n\t"
        "4:\n\t"
        PHASE_A(2048, 1056, 1584)
        PHASE_B(3072, 2112, 2640)
        PHASE_A(4096, 3168, 3696)
        PHASE_B(5120, 4224, 4752)
        "v_add_u32 v97, 0x1000, v97\n\t"
        "v_add_u32 v98, 0x1080, v98\n\t"
        "s_sub_u32 %[cnt], %[cnt], 1\n\t"
        "s_cmp_lg_u32 %[cnt], %[bar]\n\t"
        "s_cbranch_scc1 2f\n\t"
        "s_waitcnt vmcnt(0)\n\t"
        "s_barrier\n\t"
        "2:\n\t"
        "s_cmp_lg_u32 %[cnt], 0\n\t"
        "s_cbranch_scc1 1b\n\t"
        "s_waitcnt lgkmcnt(0)\n\t"
        "s_nop 15\n\t"
        : "+{v[16:31]}"(o), "+{v97}"(kaddr), "+{v98}"(vaddr), [cnt] "+s"(cnt), [tmp] "=&s"(tmp)
        : "{v[32:47]}"(cin), "{v[80:83]}"(q), "{v96}"(idx), [bar] "s"(barcnt), [par] "s"(par)
        : "v48", "v49", "v50", "v51", "v52", "v53", "v54", "v55", "v56", "v57", "v58", "v59", "v60", "v61", "v62", "v63",
          "v64", "v65", "v66", "v67", "v68", "v69", "v70", "v71", "v72", "v73", "v74", "v75", "v76", "v77", "v78", "v79",
          "v84", "v85", "v86", "v87", "v88", "v89", "v90", "v91", "v92", "v93", "v94", "v95", "scc", "memory");
}

DEV float sample_ref(LAS const char* kb, const bf16x8& q) {
    const f32x16 z = {};
    float m = max16(mfma(*(LAS const bf16x8*)kb, q, z), -3.0e38f);
    m = max16(mfma(*(LAS const bf16x8*)(kb + 1024), q, z), m);
    return halfmax(m) + MOFF;
}
struct Args {
    const uint16_t* x; const uint16_t* wq; const uint16_t* wk; const uint16_t* wv; const uint16_t* wo; const uint16_t* w1; const uint16_t* w2;
    char* ws; uint16_t* out;
};
DEV unsigned load_epoch(const char* ws) {
    unsigned epoch;
    asm volatile("s_load_dword %0, %1, 0x0\n\ts_waitcnt lgkmcnt(0)" : "=s"(epoch) : "s"(ws + WS_EPOCH) : "memory");
    return epoch;
}
DEV unsigned epoch_tag(unsigned epoch) { return epoch * 0x9E3779B1u + 0x7F4A7C15u; }
DEV void lds_add(LAS char* p) { __hip_atomic_fetch_add((LAS unsigned*)p, 1u, __ATOMIC_RELAXED, __HIP_MEMORY_SCOPE_WORKGROUP); }
DEV unsigned lds_add_rtn(LAS char* p) { return __hip_atomic_fetch_add((LAS unsigned*)p, 1u, __ATOMIC_RELAXED, __HIP_MEMORY_SCOPE_WORKGROUP); }
DEV unsigned lds_vol(LAS char* p) { return *(LAS volatile unsigned*)p; }
DEV void x_tile_dma(const uint16_t* x, int rb, int w, int l, unsigned sm0) {
#pragma unroll
    for (int i = 0; i < 4; ++i) {
        const int row = 4 * w + i;
        glds16s(x + (size_t)(rb * 64 + row) * DM, (unsigned)((l ^ (row & 15)) << 4), sm0 + L_X + row * 1024);
    }
}

DEV void g_role(const Args& A, LAS char* sm, unsigned sm0, int w, int l, int b, int rb, int j) {
    char* ws = A.ws;
    const int cu = rb * 8 + j, r32 = l & 31, h = l >> 5;
    x_tile_dma(A.x, rb, w, l, sm0);
    const int hcount = j < 3 ? 6 : 5, hstart = j < 3 ? 6 * j : 18 + 5 * (j - 3);
    const int gtype = w < 2 ? 0 : (w - 2 < hcount ? 1 : 2);
    const int krot = ROT ? 8 * (rb & 3) : 0;
    const uint16_t* wp = gtype == 0 ? A.wq + 64 * j + 32 * w : A.w1 + (hstart + (gtype == 1 ? w - 2 : 0)) * 32;
    const int ldw = gtype == 0 ? DM : FF;
    const unsigned wvo = (unsigned)((l >> 2) * ldw * 2 + (l & 3) * 16);
    u32x4 hpk[4];
    unsigned tag;
    if (gtype == 2) {
        tag = epoch_tag(load_epoch(ws));
        wait_vm<0>();
        barrier();
        if (l == 0) lds_add(sm + L_SYNC + 8);
    } else {
        const unsigned wst = sm0 + L_WR + w * (WRING * 1024);
        auto wdma = [&](int k) { glds16s(wp + (size_t)((k + krot) & 31) * 16 * ldw, wvo, wst + (k % WRING) * 1024); };
#pragma unroll
        for (int k = 0; k < WPRE; ++k) wdma(k);
        if (w == 0 && l == 0) *(LAS u32x4*)(sm + L_SYNC) = (u32x4){0u, 0u, 0u, 0u};
        tag = epoch_tag(load_epoch(ws));
        wait_vm<WPRE>();
        barrier();
        ST(1);
        f32x16 acc0 = {}, acc1 = {};
        f32x4 cs4 = {};
        LAS const char* xb = sm + L_X + r32 * 1024;
        const unsigned xo = (unsigned)((h ^ (r32 & 15)) << 4);
        LAS const char* wb = sm + L_WR + w * (WRING * 1024) + wfrag_lane_off(l);
        auto xoff = [&](int kk) -> unsigned { const unsigned kr = (unsigned)(kk + krot) & 31u; return (xo ^ ((kr & 7u) << 5)) + (kr >> 3) * 256u; };
        bf16x8 x0[2], x1[2], wf[2];
#pragma unroll
        for (int k = WPRE; k < WRING - 1; ++k) wdma(k);
        wait_vm<WRING - 2>();
        { const unsigned off = xoff(0); wf[0] = wfrag(wb); x0[0] = *(LAS const bf16x8*)(xb + off); x1[0] = *(LAS const bf16x8*)(xb + 32768 + off); }
        sfor<32>([&](auto K) {
            constexpr int k = K.v;
            if constexpr (k + WRING - 1 < 32) wdma(k + WRING - 1);
            if constexpr (k + 1 < 32) {
                constexpr int last = (k + WRING - 1 < 32) ? k + WRING - 1 : 31;
                wait_vm<last - (k + 1)>();
                constexpr int kk = k + 1;
                const unsigned off = xoff(kk);
                wf[kk & 1] = wfrag(wb + (kk % WRING) * 1024);
                x0[kk & 1] = *(LAS const bf16x8*)(xb + off); x1[kk & 1] = *(LAS const bf16x8*)(xb + 32768 + off);
            }
            SB();
            acc0 = mfma(wf[k & 1], x0[k & 1], acc0); acc1 = mfma(wf[k & 1], x1[k & 1], acc1);
            if (EXP_CS == 0) cs4 = lanesum8(__builtin_bit_cast(u32x4, wf[k & 1]), cs4);
            SB();
        });
        ST(2);
        if (l == 0) lds_add(sm + L_SYNC + 8);
        const float cs = xsum32(cs4[0]);
        LAS float* csb = (LAS float*)(sm + L_CS + w * 128);
        if (l < 32) csb[l] = cs;
        { unsigned sp = 0; while (lds_vol(sm + L_SYNC) < 4u || lds_vol(sm + L_SYNC + 4) < 2u) { __builtin_amdgcn_s_sleep(1); if (++sp > SPIN_CAP) break; } }
        wait_lgkm0();
        const f32x2 st0 = *(LAS const f32x2*)(sm + L_STATS + r32 * 8), st1 = *(LAS const f32x2*)(sm + L_STATS + (32 + r32) * 8);
        f32x4 csv[4];
#pragma unroll
        for (int i = 0; i < 4; ++i) csv[i] = *(LAS const f32x4*)(csb + 8 * i + 4 * h);
#pragma unroll
        for (int rt = 0; rt < 2; ++rt) {
            const f32x16& a = rt ? acc1 : acc0;
            const float mean = rt ? st1[0] : st0[0], rs = rt ? st1[1] : st0[1];
            float v[16];
#pragma unroll
            for (int i = 0; i < 16; ++i) v[i] = (a[i] - mean * csv[i >> 2][i & 3]) * rs;
            if (gtype == 0) {
                const float sc = 0.25f * 1.4426950408889634f;
#pragma unroll
                for (int p = 0; p < 2; ++p) {
                    u32x2 g0 = {pk2(v[8 * p] * sc, v[8 * p + 1] * sc), pk2(v[8 * p + 2] * sc, v[8 * p + 3] * sc)};
                    u32x2 g1 = {pk2(v[8 * p + 4] * sc, v[8 * p + 5] * sc), pk2(v[8 * p + 6] * sc, v[8 * p + 7] * sc)};
                    *(LAS u32x4*)(sm + L_Q + (rt * 4 + 2 * w + p) * 1024 + l * 16) = widen(g0, g1);
                }
            } else {
#pragma unroll
                for (int p = 0; p < 2; ++p) {
                    u32x2 g0 = {pk2(fmaxf(v[8 * p], 0.f), fmaxf(v[8 * p + 1], 0.f)), pk2(fmaxf(v[8 * p + 2], 0.f), fmaxf(v[8 * p + 3], 0.f))};
                    u32x2 g1 = {pk2(fmaxf(v[8 * p + 4], 0.f), fmaxf(v[8 * p + 5], 0.f)), pk2(fmaxf(v[8 * p + 6], 0.f), fmaxf(v[8 * p + 7], 0.f))};
                    hpk[rt * 2 + p] = widen(g0, g1);
                }
            }
        }
        ST(3);
    }
    barrier();
    ST(4);
    if (w == 0 && l < 12) *(LAS unsigned*)(sm + L_SYNC2 + 4 * l) = 0u;
    barrier();
    ST(5);
    {
        const char* src = ws + WS_KV; const unsigned lo = l * 16;
#pragma unroll
        for (int i = 0; i < 6; ++i) {
            glds16s_sc1(src + (16 + w + 8 * i) * 1024, lo, sm0 + (16 + w + 8 * i) * 1024);
            glds16s_sc1(src + (80 + w + 8 * i) * 1024, lo, sm0 + (80 + w + 8 * i) * 1024);
            if (GPACE && i < 5) __builtin_amdgcn_s_sleep(GPACE);
        }

        if (w < 2 && !EXP_NOGATHER) glds16s_sc1(src + (128 + w) * 1024, lo, sm0 + (128 + w) * 1024);
        if (gtype == 1 && !EXP_NOHST) {
            char* hb = ws + WS_H + ((size_t)(rb * 2) * HSTEPS + 2 * (hstart + w - 2)) * 1024 + l * 16;
#pragma unroll
            for (int rt = 0; rt < 2; ++rt)
#pragma unroll
                for (int p = 0; p < 2; ++p) st16_dev(hb + ((size_t)rt * HSTEPS + p) * 1024, hpk[rt * 2 + p]);
            wait_vm<4>();
        } else wait_vm<0>();
    }
    ST(7);
    barrier();
    if (ALTPRIO) __builtin_amdgcn_s_setprio(GPRIO);
    ST(15);
    const int nt = w & 1, kq = w >> 1;
    const int k0 = kq < 2 ? 22 * kq : 44 + 21 * (kq - 2), nk = kq < 2 ? 22 : 21;
    const char* hp = ws + WS_H + ((size_t)(rb * 2) * HSTEPS + k0) * 1024;
    const uint16_t* w2p = A.w2 + (size_t)(k0 * 16) * DM + 64 * j + 32 * nt;
    wait_vm<0>();
    if (l == 0) { if (lds_add_rtn(sm + L_SYNC2 + 4) == 7u) st4_dev(ws + WS_FH + rb * 128 + j * 4, tag); }
    ST(14);
    u32x4 pf0, pf1;
    if (L2PF) {
        const unsigned wv = (unsigned)((l >> 2) * DM * 2 + (l & 3) * 16);
        const uint16_t* wop = A.wo + (size_t)(kq * 128) * DM + 64 * j + 32 * nt;
#pragma unroll
        for (int s = 0; s < 8; ++s) { if (s & 1) ld16s_async(pf1, wop + (size_t)s * 16 * DM, wv); else ld16s_async(pf0, wop + (size_t)s * 16 * DM, wv); }
        if (L2PF > 1) {
#pragma unroll
            for (int s = 0; s < 21; ++s) { if (s & 1) ld16s_async(pf1, w2p + (size_t)s * 16 * DM, wv); else ld16s_async(pf0, w2p + (size_t)s * 16 * DM, wv); }
        }
    }
    if (w == 0) {
        for (unsigned sp = 0; sp < SPIN_CAP; ++sp) {
            const unsigned f = ld4_dev(ws + WS_FH + rb * 128 + (l & 7) * 4);
            if (__all(f == tag)) break;
            __builtin_amdgcn_s_sleep(HPOLL_SLEEP);
        }
        if (HACQ) { __builtin_amdgcn_fence(__ATOMIC_ACQUIRE, "agent"); wait_vm<0>(); }
        if (l == 0) *(LAS volatile unsigned*)(sm + L_SYNC2) = 1u;
    } else {
        unsigned sp = 0; while (lds_vol(sm + L_SYNC2) == 0u) { __builtin_amdgcn_s_sleep(8); if (++sp > SPIN_CAP) break; }
    }
    ST(13);
    f32x16 acc0 = {}, acc1 = {};
    {
        const int krot2 = ROT ? (XMAP ? (5 * (j & 3) + 3 * (rb & 7)) % 21 : (5 * (rb & 3) + 3 * j) % 21) : 0;
        const unsigned wvo2 = (unsigned)((l >> 2) * DM * 2 + (l & 3) * 16), lo = l * 16;
        LAS char* rsl = sm + L_RING2 + w * 3072;
        LAS const char* rb2 = rsl + wfrag_lane_off(l);
        u32x4 wr[FDW], xa[FDX], xb[FDX];
        auto kidx = [&](int k) { int kc = (k < 21 || k < nk) ? k : 20; kc += krot2; if (kc >= nk) kc -= nk; return kc; };
        auto issueW = [&](auto K) { constexpr int k = K.v; ld16s_async(wr[k % FDW], w2p + (size_t)kidx(k) * 16 * DM, wvo2); };
        auto issueX = [&](auto K) { constexpr int k = K.v; const int kc = kidx(k); if (HACQ) { ld16s_async(xa[k % FDX], hp + (size_t)kc * 1024, lo); ld16s_async(xb[k % FDX], hp + (size_t)(HSTEPS + kc) * 1024, lo); } else { ld16s_dev_async(xa[k % FDX], hp + (size_t)kc * 1024, lo); ld16s_dev_async(xb[k % FDX], hp + (size_t)(HSTEPS + kc) * 1024, lo); } };
        constexpr int DW = FDW - FDX;
        sfor<DW>([&](auto K) { issueW(K); });
        sfor<FDX>([&](auto K) { issueX(K); if constexpr (K.v + DW < 22) issueW(IC<K.v + DW>{}); });
        static_assert(DW >= 2, "W must run at least two steps ahead of X");
        auto younger = [](int k) constexpr { int n = (DW >= 3 && k + DW < 22) ? 1 : 0; for (int g = k + 1; g <= k + FDX - 1 && g < 22; ++g) n += 2 + ((g + DW < 22) ? 1 : 0); return n; };
        wait_vm<younger(0)>(); PIN(wr[0]); PIN(wr[1]);
        if (L2PF) { PIN(pf0); PIN(pf1); }
        *(LAS u32x4*)(rsl + l * 16) = wr[0]; *(LAS u32x4*)(rsl + 1024 + l * 16) = wr[1];
        bf16x8 wf[2];
        wf[0] = wfrag(rb2);
        sfor<22>([&](auto K) {
            constexpr int k = K.v;
            if constexpr (k >= 1) wait_vm<younger(k)>();
            if constexpr (k + 2 < 22) { PIN(wr[(k + 2) % FDW]); *(LAS u32x4*)(rsl + ((k + 2) % 3) * 1024 + l * 16) = wr[(k + 2) % FDW]; }
            if constexpr (k + 1 < 22) wf[(k + 1) & 1] = wfrag(rb2 + ((k + 1) % 3) * 1024);
            PIN(xa[k % FDX]); PIN(xb[k % FDX]);
            SB();
            if (k < 21 || k < nk) { acc0 = mfma(wf[k & 1], __builtin_bit_cast(bf16x8, xa[k % FDX]), acc0); acc1 = mfma(wf[k & 1], __builtin_bit_cast(bf16x8, xb[k % FDX]), acc1); }
            SB();
            if constexpr (k + FDX < 22) issueX(IC<k + FDX>{});
            if constexpr (k + FDW < 22) issueW(IC<k + FDW>{});
            if constexpr (k == KSPLIT - 1) { ST(6); barrier(); ST(8); }
        });
    }
    const int fnt = w & 1, frt = (w >> 1) & 1, fp = (w >> 2) & 1;
    const size_t ro = (size_t)(rb * 64 + frt * 32 + r32) * DM + 64 * j + 32 * fnt + 16 * fp;
    const u32x2 x0 = *(const u32x2*)(A.x + ro + 4 * h), x1 = *(const u32x2*)(A.x + ro + 8 + 4 * h);
    if (KSPLIT > 22) { ST(6); barrier(); ST(8); }
    ST(7);
    {
        const uint16_t* wop = A.wo + (size_t)(kq * 128) * DM + 64 * j + 32 * nt;
        const unsigned wvo3 = (unsigned)((l >> 2) * DM * 2 + (l & 3) * 16);
#pragma unroll
        for (int s = 0; s < 8; ++s) glds16s(wop + (size_t)s * 16 * DM, wvo3, sm0 + L_WO + w * 8192 + s * 1024);
        if (w == 0) {
            for (unsigned sp = 0; sp < SPIN_CAP; ++sp) {
                const unsigned f = ld4_dev(ws + WS_FO + rb * 128 + (l & 7) * 4);
                if (__all(f == tag)) break;
                __builtin_amdgcn_s_sleep(OPOLL_SLEEP);
            }
        }
    }
    barrier();
    ST(9);
    {
        u32x4 oa[8], ob[8];
        const char* op = ws + WS_O + ((size_t)(rb * 2) * 32 + 8 * kq) * 1024;
#pragma unroll
        for (int s = 0; s < 8; ++s) { ld16s_dev_async(oa[s], op + s * 1024, l * 16); ld16s_dev_async(ob[s], op + (32 + s) * 1024, l * 16); }
        LAS const char* wb = sm + L_WO + w * 8192 + wfrag_lane_off(l);
        wait_vm<16>();
        bf16x8 wfo[2];
        wfo[0] = wfrag(wb);
        sfor<8>([&](auto S) {
            constexpr int s = S.v;
            if constexpr (s + 1 < 8) wfo[(s + 1) & 1] = wfrag(wb + (s + 1) * 1024);
            wait_vm<14 - 2 * s>(); PIN(oa[s]); PIN(ob[s]);
            SB();
            acc0 = mfma(wfo[s & 1], __builtin_bit_cast(bf16x8, oa[s]), acc0); acc1 = mfma(wfo[s & 1], __builtin_bit_cast(bf16x8, ob[s]), acc1);
            SB();
        });
        LAS f32x4* red = (LAS f32x4*)(sm + L_RED + w * 8192) + l;
#pragma unroll
        for (int i = 0; i < 4; ++i) {
            red[i * 64] = (f32x4){acc0[4 * i], acc0[4 * i + 1], acc0[4 * i + 2], acc0[4 * i + 3]};
            red[(4 + i) * 64] = (f32x4){acc1[4 * i], acc1[4 * i + 1], acc1[4 * i + 2], acc1[4 * i + 3]};
        }
    }
    ST(10);
    barrier();
    {
        f32x4 v0 = {}, v1 = {};
#pragma unroll
        for (int q4 = 0; q4 < 4; ++q4) {
            LAS const f32x4* red = (LAS const f32x4*)(sm + L_RED + (fnt + 2 * q4) * 8192) + l;
            v0 += red[(frt * 4 + 2 * fp) * 64]; v1 += red[(frt * 4 + 2 * fp + 1) * 64];
        }
        u32x2 g0 = {pk2(v0[0] + bflo(x0[0]), v0[1] + bfhi(x0[0])), pk2(v0[2] + bflo(x0[1]), v0[3] + bfhi(x0[1]))};
        u32x2 g1 = {pk2(v1[0] + bflo(x1[0]), v1[1] + bfhi(x1[0])), pk2(v1[2] + bflo(x1[1]), v1[3] + bfhi(x1[1]))};
        *(u32x4*)(A.out + ro + 8 * h) = widen(g0, g1);
    }
    ST(11);
}

DEV void a_role(const Args& A, LAS char* sm, unsigned sm0, int w, int l, int b, int rb, int j) {
    char* ws = A.ws;
    const int cu = rb * 8 + j, r32 = l & 31, h = l >> 5, a = w - 8, c16 = l & 15, g = l >> 4;
    x_tile_dma(A.x, rb, w, l, sm0);
    {
        const uint16_t* src = (a < 4 ? A.wk : A.wv) + (a & 3) * 2048;
#pragma unroll
        for (int i = 0; i < 4; ++i) glds16s(src + i * 512, l * 16, sm0 + (a < 4 ? L_WK : L_WV) + (a & 3) * 4096 + i * 1024);
    }
    unsigned tlb0 = 0, tlb1 = 0;
    if (TLBW && a == 3) {
        const char* p0; unsigned o0;
        if (l < 22) { p0 = (const char*)A.w1; o0 = l * 65536; }
        else if (l < 44) { p0 = (const char*)A.w2; o0 = (l - 22) * 65536; }
        else if (l < 52) { p0 = (const char*)A.wq; o0 = (l - 44) * 65536; }
        else if (l < 60) { p0 = (const char*)A.wo; o0 = (l - 52) * 65536; }
        else { p0 = (const char*)A.out; o0 = (rb * 64 + (l - 60) * 16) * 1024; }
        const char* p1 = ws; unsigned o1;
        if (l < 3) o1 = WS_KV + l * 65536;
        else if (l < 7) o1 = WS_H + (unsigned)(rb * 2 + ((l - 3) >> 1)) * HSTEPS * 1024 + ((l - 3) & 1) * 65536;
        else if (l < 9) o1 = WS_O + (unsigned)(rb * 2 + (l - 7)) * 32 * 1024;
        else o1 = WS_FK + ((l - 9) & 3) * 4096;
        asm volatile("global_load_dword %0, %1, off" : "=v"(tlb0) : "v"(p0 + o0) : "memory");
        asm volatile("global_load_dword %0, %1, off" : "=v"(tlb1) : "v"(p1 + o1) : "memory");
    }
    const unsigned epoch = load_epoch(ws), tag = epoch_tag(epoch);
    if (TLBW && a == 3) wait_vm<2>(); else wait_vm<0>();
    barrier();
    ST(1);
    if (TLBW && a == 3) { wait_vm<0>(); PIN(tlb0); PIN(tlb1); }
    __builtin_amdgcn_s_setprio(3);
    bf16x8 ones; for (int e = 0; e < 8; ++e) ones[e] = (__bf16)1.0f;
    if (a < 2) {
        const int g16 = j >> 1, xrow = 16 * g16 + c16;
        LAS const char* xb = sm + L_X + xrow * 1024;
        LAS const char* wb = sm + (a == 0 ? L_WK : L_WV) + (8 * g + (c16 >> 2)) * 32 + (l & 3) * 8;
        f32x4 d = {}, cs = {};
        auto chain = [&](auto AA) {
#pragma unroll
            for (int bt = 0; bt < 2; ++bt) {
                bf16x8 xf[8], wf[8];
#pragma unroll
                for (int s = 0; s < 8; ++s) {
                    const int ss = 8 * bt + s;
                    xf[s] = *(LAS const bf16x8*)(xb + (((4 * ss + g) ^ c16) << 4));
                    wf[s] = cat8(tr_read(wb + ss * 1024), tr_read(wb + ss * 1024 + 128));
                }
                SB();
#pragma unroll
                for (int s = 0; s < 8; ++s) {
                    if constexpr (AA.v == 0) { d = mfma16(wf[s], xf[s], d); cs = mfma16(wf[s], ones, cs); } else { d = mfma16(xf[s], wf[s], d); cs = mfma16(ones, wf[s], cs); }
                }
                SB();
            }
        };
        if (a == 0) chain(IC<0>{}); else chain(IC<1>{});
        PIN(d); PIN(cs);
        if (l == 0) lds_add(sm + L_SYNC + 12);
        { unsigned sp = 0; while (lds_vol(sm + L_SYNC) < 4u) { __builtin_amdgcn_s_sleep(1); if (++sp > SPIN_CAP) break; } }
        const int key = rb * 64 + 16 * g16;
        char* kv = ws + WS_KV;
        if (a == 0) {
            const f32x2 st = *(LAS const f32x2*)(sm + L_STATS + xrow * 8);
            const float mean = st[0], rs = st[1];
            u32x2 o = {pk2((d[0] - mean * cs[0]) * rs, (d[1] - mean * cs[1]) * rs), pk2((d[2] - mean * cs[2]) * rs, (d[3] - mean * cs[3]) * rs)};
            const int kk = key + c16;
            if ((c16 >> 3) == (j & 1)) st8_dev(kv + (kk >> 5) * 1024 + (g >> 1) * 512 + (kk & 31) * 16 + (g & 1) * 8, o);
        } else {
            float v[4];
#pragma unroll
            for (int r = 0; r < 4; ++r) { const f32x2 st = *(LAS const f32x2*)(sm + L_STATS + (16 * g16 + 4 * g + r) * 8); v[r] = (d[r] - st[0] * cs[r]) * st[1]; }
            u32x2 o = {pk2(v[0], v[1]), pk2(v[2], v[3])};
            char* vb = kv + KIMG + (key >> 4) * VSTEP;
            if ((g >> 1) == (j & 1)) st8_dev(vb + (j & 1) * 256 + c16 * 16 + (g & 1) * 8, o);
            if ((j & 1) == 0 && l < 2) st8_dev(vb + 512 + l * 8, (u32x2){0x3F803F80u, 0x3F803F80u});
        }
        wait_vm<0>();
        if (l == 0) { if (lds_add_rtn(sm + L_SYNC + 4) == 1u) st4_dev(ws + WS_FK + (KSPREAD ? rb * 128 + j * 4 : cu * 4), tag); }
        ST(2);
    } else if (a >= 4) {
        const int xrow = 16 * (a - 4) + c16;
        LAS const char* xb = sm + L_X + xrow * 1024;
        f32x4 m1 = {}, m2 = {};
        bf16x8 xf[16];
#pragma unroll
        for (int s = 0; s < 16; ++s) xf[s] = *(LAS const bf16x8*)(xb + (((4 * s + g) ^ c16) << 4));
        SB();
#pragma unroll
        for (int s = 0; s < 16; ++s) { m1 = mfma16(ones, xf[s], m1); m2 = mfma16(xf[s], xf[s], m2); }
        const float dg = (c16 & 2) ? ((c16 & 1) ? m2[3] : m2[2]) : ((c16 & 1) ? m2[1] : m2[0]);
        const float s2 = __shfl(dg, 16 * (c16 >> 2) + c16);
        const float mean = m1[0] * (1.f / DM), rs = __builtin_amdgcn_rsqf(fmaxf(s2 * (1.f / DM) - mean * mean, 0.f) + 1e-5f);
        if (l < 16) *(LAS f32x2*)(sm + L_STATS + xrow * 8) = (f32x2){mean, rs};
        wait_lgkm0();
        if (l == 0) lds_add(sm + L_SYNC);
        ST(2);
    } else if (a == 2) {
        { unsigned sp = 0; while (lds_vol(sm + L_SYNC + 8) < 8u) { __builtin_amdgcn_s_sleep(4); if (++sp > SPIN_CAP) break; } }
        const __amdgpu_buffer_rsrc_t rs = __builtin_amdgcn_make_buffer_rsrc((void*)(ws + WS_FK), 0, 4096, 0x00020000);
        for (unsigned sp = 0; sp < SPIN_CAP; ++sp) {
            asm volatile("" ::: "memory");
            const u32x4 fa = __builtin_amdgcn_raw_buffer_load_b128(rs, KSPREAD ? (l & 31) * 128 + (l >> 5) * 16 : l * 16, 0, 16);
            const bool ok = fa[0] == tag && fa[1] == tag && fa[2] == tag && fa[3] == tag;
            if (__all(ok)) break;
            __builtin_amdgcn_s_sleep(POLL_SLEEP);
        }
        if (b == 0 && l == 0) st4_dev(ws + WS_EPOCH, epoch + 1u);
        ST(2);
    }
    barrier();
    ST(4);
    bf16x8 q;
    {
        const char* src = ws + WS_KV; const unsigned lo = l * 16;
        glds16s_sc1(src + a * 1024, lo, sm0 + a * 1024);
        glds16s_sc1(src + (64 + a) * 1024, lo, sm0 + (64 + a) * 1024);
        glds16s_sc1(src + (8 + a) * 1024, lo, sm0 + (8 + a) * 1024);
        glds16s_sc1(src + (72 + a) * 1024, lo, sm0 + (72 + a) * 1024);
        q = *(LAS const bf16x8*)(sm + L_Q + a * 1024 + l * 16);
        wait_vm<0>();
    }
    barrier();
    ST(5);
    const int head = 4 * j + (a & 3), qh = a >> 2;
    __builtin_amdgcn_s_setprio(APRIO);
    LAS const char* kb = sm + l * 16;
    LAS const char* vb = sm + KIMG + (r32 < 16 ? h * 256 + r32 * 16 : 512);
    f32x16 o;
    {
        float mref = sample_ref(kb, q);
        for (int attempt = 0;; ++attempt) {
            f32x16 cin;
#pragma unroll
            for (int i = 0; i < 16; ++i) { cin[i] = -mref; o[i] = 0.f; }
            attn_loop(o, cin, q, (unsigned)(uintptr_t)kb, (unsigned)(uintptr_t)vb, 16, attempt == 0 ? 14 : -1, (a >> 2) << ALTBIT);
            bool bad = !(o[8] < 1.0e30f) || !(o[8] > 0.f);
#pragma unroll
            for (int i = 0; i < 8; ++i) bad = bad || !(fabsf(o[i]) < 3.0e38f);
            if (attempt > 0 || !__any(bad)) break;
            float m = -3.0e38f; const f32x16 z = {};
            for (int t = 0; t < 64; ++t) m = max16(mfma(*(LAS const bf16x8*)(kb + t * 1024), q, z), m);
            mref = halfmax(m);
        }
    }
    ST(6);
    const float r = 1.0f / o[8];
    u32x2 g0 = {pk2(o[0] * r, o[1] * r), pk2(o[2] * r, o[3] * r)}, g1 = {pk2(o[4] * r, o[5] * r), pk2(o[6] * r, o[7] * r)};
    st16_dev(ws + WS_O + ((size_t)(rb * 2 + qh) * 32 + head) * 1024 + l * 16, widen(g0, g1));
    barrier();
    ST(8);
    wait_vm<0>();
    if (l == 0) { if (lds_add_rtn(sm + L_SYNC2 + 8) == 7u) st4_dev(ws + WS_FO + rb * 128 + j * 4, tag); }
    ST(7);
    barrier();
    barrier();
    ST(11);
}

__global__ __launch_bounds__(1024, 4) void fused_kernel(const uint16_t* __restrict__ x, const uint16_t* __restrict__ wq, const uint16_t* __restrict__ wk,
                                                        const uint16_t* __restrict__ wv, const uint16_t* __restrict__ wo, const uint16_t* __restrict__ w1,
                                                        const uint16_t* __restrict__ w2, char* __restrict__ ws, uint16_t* __restrict__ out) {
    __shared__ __attribute__((aligned(16))) char smem[LDS_BYTES];
    const int tid = threadIdx.x, l = tid & 63, w = __builtin_amdgcn_readfirstlane(tid >> 6);
    const int b = blockIdx.x, xcd = b & 7, idx = b >> 3;
    const int rb = XMAP ? (xcd >> 1) * 8 + (idx >> 2) : xcd * 4 + (idx >> 3), j = XMAP ? (xcd & 1) * 4 + (idx & 3) : idx & 7;
    const unsigned sm0 = (unsigned)(uintptr_t)smem;
    LAS char* sm = (LAS char*)smem;
    ST(0);
    const Args A = {x, wq, wk, wv, wo, w1, w2, ws, out};
    if (w < 8) g_role(A, sm, sm0, w, l, b, rb, j); else a_role(A, sm, sm0, w, l, b, rb, j);
}

extern "C" void kernel_launch(void* const* d_in, const int* in_sizes, int n_in, void* d_out, int out_size, void* d_ws, size_t ws_size,
                              hipStream_t stream) {
    fused_kernel<<<256, 1024, 0, stream>>>((const uint16_t*)d_in[0], (const uint16_t*)d_in[1], (const uint16_t*)d_in[2], (const uint16_t*)d_in[3],
                                           (const uint16_t*)d_in[4], (const uint16_t*)d_in[6], (const uint16_t*)d_in[7], (char*)d_ws, (uint16_t*)d_out);
}
```

```cpp
#include <hip/hip_runtime.h>
#include <hip/hip_bf16.h>
#include <stdint.h>

typedef __attribute__((ext_vector_type(8))) __bf16 bf16x8;
typedef __attribute__((ext_vector_type(2))) __bf16 bf16x2;
typedef __attribute__((ext_vector_type(16))) float f32x16;
typedef __attribute__((ext_vector_type(4))) float f32x4;
typedef __attribute__((ext_vector_type(2))) float f32x2;
typedef __attribute__((ext_vector_type(4))) unsigned u32x4;
typedef __attribute__((ext_vector_type(2))) unsigned u32x2;
typedef __attribute__((ext_vector_type(4))) short s16x4;
typedef __attribute__((ext_vector_type(8))) short s16x8;
#define LAS __attribute__((address_space(3)))
#define GAS __attribute__((address_space(1)))
#define DEV __device__ __forceinline__
#define SB() __builtin_amdgcn_sched_barrier(0)

constexpr int SEQ = 2048, DM = 512, NH = 32, DH = 16, FF = 1376;
constexpr int KIMG = 65536, VSTEP = 528, VIMG = 128 * VSTEP, KVIMG = KIMG + VIMG;
constexpr int HSTEPS = FF / 16;
constexpr size_t WS_EPOCH = 0, WS_FK = 4096, WS_FH = 8192, WS_FO = 16384;
constexpr size_t WS_KV = 65536, WS_O = 1u << 20, WS_H = WS_O + (2u << 20);
constexpr size_t WS_DBG = 64u << 20;
constexpr int LDS_BYTES = 163840;
constexpr int L_X = 0, L_WR = 65536, WRING = 7, L_MISC = 122880;
constexpr int L_STATS = L_MISC, L_CS = L_MISC + 512, L_SYNC = L_MISC + 1536;
constexpr int L_WK = 131072, L_WV = 147456;
constexpr int L_Q = 133120, L_RING2 = 133120;
constexpr int L_SYNC2 = L_RING2 + 8 * 3072;
constexpr int L_WO = 0, L_RED = 65536;
#define MOFF 96.f
#define WPRE 2
#define WD 8
#define OPOLL_SLEEP 2
#define EXP_CS 0
#define ROT 1
#define FD 5
#define XMAP 1
#define GWAIT 0
#define CODEPF 0
constexpr int CODE_KB = 26;
#define CODE_BACK 0
#define EXP_NOFFN2 0
#define NA 52
#define GS1 2
#define TOUCH 0
#define EXP_NOGATHER 0
#define EXP_NOHST 0
#define FDX 5
#define FDW 8
#define HACQ 1
#define HPOLL_SLEEP 16
#define GPACE 4
#define KSPLIT 13
#define KSPREAD 0
#define APRIO 0
#define POLL_SLEEP 8
constexpr unsigned SPIN_CAP = 1u << 20;

#define ST(i)

template <int... I> struct ISeq {};
template <int N, int... I> struct MkSeq : MkSeq<N - 1, N - 1, I...> {};
template <int... I> struct MkSeq<0, I...> { typedef ISeq<I...> T; };
template <int V> struct IC { static constexpr int v = V; };
template <class F, int... I> DEV void sfor_(F&& f, ISeq<I...>) { (f(IC<I>{}), ...); }
template <int N, class F> DEV void sfor(F&& f) { sfor_(f, typename MkSeq<N>::T{}); }

DEV unsigned pk2(float lo, float hi) { f32x2 v = {lo, hi}; return __builtin_bit_cast(unsigned, __builtin_convertvector(v, bf16x2)); }
DEV float bflo(unsigned u) { return __uint_as_float(u << 16); }
DEV float bfhi(unsigned u) { return __uint_as_float(u & 0xffff0000u); }
DEV f32x16 mfma(bf16x8 a, bf16x8 b, f32x16 c) { return __builtin_amdgcn_mfma_f32_32x32x16_bf16(a, b, c, 0, 0, 0); }
DEV f32x4 mfma16(bf16x8 a, bf16x8 b, f32x4 c) { return __builtin_amdgcn_mfma_f32_16x16x32_bf16(a, b, c, 0, 0, 0); }
DEV void glds16(const void* g, unsigned lds_addr) {
    unsigned keep;
    asm volatile("s_mov_b32 %0, m0\n\ts_mov_b32 m0, %2\n\ts_nop 2\n\tglobal_load_lds_dwordx4 %1, off\n\ts_mov_b32 m0, %0"
                 : "=&s"(keep) : "v"(g), "s"(lds_addr) : "memory");
}
DEV void glds16_sc1(const void* g, unsigned lds_addr) {
    unsigned keep;
    asm volatile("s_mov_b32 %0, m0\n\ts_mov_b32 m0, %2\n\ts_nop 2\n\tglobal_load_lds_dwordx4 %1, off sc1\n\ts_mov_b32 m0, %0"
                 : "=&s"(keep) : "v"(g), "s"(lds_addr) : "memory");
}
DEV void glds16s(const void* sbase, unsigned voff, unsigned lds_addr) {
    unsigned keep;
    asm volatile("s_mov_b32 %0, m0\n\ts_mov_b32 m0, %3\n\ts_nop 2\n\tglobal_load_lds_dwordx4 %1, %2\n\ts_mov_b32 m0, %0"
                 : "=&s"(keep) : "v"(voff), "s"(sbase), "s"(lds_addr) : "memory");
}
DEV void glds16s_sc1(const void* sbase, unsigned voff, unsigned lds_addr) {
    unsigned keep;
    asm volatile("s_mov_b32 %0, m0\n\ts_mov_b32 m0, %3\n\ts_nop 2\n\tglobal_load_lds_dwordx4 %1, %2 sc1\n\ts_mov_b32 m0, %0"
                 : "=&s"(keep) : "v"(voff), "s"(sbase), "s"(lds_addr) : "memory");
}
DEV void ld16s_dev_async(u32x4& d, const void* sbase, unsigned voff) { asm volatile("s_nop 4\n\tglobal_load_dwordx4 %0, %1, %2 sc1" : "=v"(d) : "v"(voff), "s"(sbase) : "memory"); }
DEV void ld16s_async(u32x4& d, const void* sbase, unsigned voff) { asm volatile("s_nop 4\n\tglobal_load_dwordx4 %0, %1, %2" : "=v"(d) : "v"(voff), "s"(sbase) : "memory"); }
DEV void touch_async(unsigned& d, const void* sbase, unsigned voff) { asm volatile("s_nop 4\n\tglobal_load_dword %0, %1, %2 sc1" : "=v"(d) : "v"(voff), "s"(sbase) : "memory"); }
DEV f32x4 lanesum8(u32x4 u, f32x4 c) {
    const s16x4 ones = {0x3F80, 0x3F80, 0x3F80, 0x3F80};
    c = __builtin_amdgcn_mfma_f32_4x4x4bf16_1k(ones, __builtin_bit_cast(s16x4, (u32x2){u[0], u[1]}), c, 0, 0, 0);
    return __builtin_amdgcn_mfma_f32_4x4x4bf16_1k(ones, __builtin_bit_cast(s16x4, (u32x2){u[2], u[3]}), c, 0, 0, 0);
}
template <int N> DEV void wait_vm() { asm volatile("s_waitcnt vmcnt(%0)" :: "n"(N) : "memory"); }
DEV void wait_lgkm0() { asm volatile("s_waitcnt lgkmcnt(0)" ::: "memory"); }
DEV void barrier() { asm volatile("s_waitcnt lgkmcnt(0)\n\ts_barrier" ::: "memory"); }
DEV s16x4 tr_read(LAS const char* p) { return __builtin_amdgcn_ds_read_tr16_b64_v4i16((LAS s16x4*)p); }
DEV bf16x8 cat8(s16x4 a, s16x4 b) { s16x8 r = {a[0], a[1], a[2], a[3], b[0], b[1], b[2], b[3]}; return __builtin_bit_cast(bf16x8, r); }
DEV bf16x8 wfrag(LAS const char* base_lane) { return cat8(tr_read(base_lane), tr_read(base_lane + 256)); }
DEV unsigned wfrag_lane_off(int l) { int h = l >> 5, blk = (l >> 4) & 1, q = (l & 15) >> 2, p = l & 3; return (8 * h + q) * 64 + blk * 32 + p * 8; }
DEV u32x4 widen(u32x2 a, u32x2 b) {
    auto r0 = __builtin_amdgcn_permlane32_swap(a[0], b[0], false, false);
    auto r1 = __builtin_amdgcn_permlane32_swap(a[1], b[1], false, false);
    u32x4 o = {r0[0], r1[0], r0[1], r1[1]};
    return o;
}
DEV float dot2(unsigned a, unsigned b, float c) { return __builtin_amdgcn_fdot2_f32_bf16(__builtin_bit_cast(bf16x2, a), __builtin_bit_cast(bf16x2, b), c, false); }
DEV float xsum16_32(float v) {
    auto a = __builtin_amdgcn_permlane16_swap(__float_as_uint(v), __float_as_uint(v), false, false);
    v = __uint_as_float(a[0]) + __uint_as_float(a[1]);
    auto b = __builtin_amdgcn_permlane32_swap(__float_as_uint(v), __float_as_uint(v), false, false);
    return __uint_as_float(b[0]) + __uint_as_float(b[1]);
}
DEV float xsum32(float v) { auto b = __builtin_amdgcn_permlane32_swap(__float_as_uint(v), __float_as_uint(v), false, false); return __uint_as_float(b[0]) + __uint_as_float(b[1]); }
DEV float halfmax(float m) { auto r = __builtin_amdgcn_permlane32_swap(__float_as_uint(m), __float_as_uint(m), false, false); return fmaxf(__uint_as_float(r[0]), __uint_as_float(r[1])); }
DEV float max16(const f32x16& s, float m) {
#pragma unroll
    for (int i = 0; i < 16; i += 2) m = fmaxf(fmaxf(m, s[i]), s[i + 1]);
    return m;
}
DEV void st16_dev(void* p, u32x4 v) { asm volatile("global_store_dwordx4 %0, %1, off sc1\n\ts_nop 1" :: "v"(p), "v"(v) : "memory"); }
DEV void st8_dev(void* p, u32x2 v) { __hip_atomic_store((GAS unsigned long long*)p, ((unsigned long long)v[1] << 32) | v[0], __ATOMIC_RELAXED, __HIP_MEMORY_SCOPE_AGENT); }
DEV void st4_dev(void* p, unsigned v) { __hip_atomic_store((GAS unsigned*)p, v, __ATOMIC_RELAXED, __HIP_MEMORY_SCOPE_AGENT); }
DEV unsigned ld4_dev(const void* p) { return __hip_atomic_load((GAS const unsigned*)p, __ATOMIC_RELAXED, __HIP_MEMORY_SCOPE_AGENT); }
DEV void ld16_dev_async(u32x4& d, const void* p) { asm volatile("global_load_dwordx4 %0, %1, off sc1" : "=v"(d) : "v"(p) : "memory"); }
#define PIN(x) asm volatile("" : "+v"(x))
DEV unsigned lds_ld_volatile(unsigned off_ptr) { return *(LAS volatile unsigned*)(uintptr_t)off_ptr; }

#define EX(r) "v_exp_f32 v" #r ", v" #r "\n\t"
#define WL2 "s_waitcnt lgkmcnt(2)\n\t"
#define QK(d0, d1) "v_mfma_f32_32x32x16_bf16 v[" #d0 ":" #d1 "], v[84:87], v[80:83], v[32:47]\n\t"
#define PV(vf0, vf1, b0, b1) "v_smfmac_f32_32x32x32_bf16 v[16:31], v[" #vf0 ":" #vf1 "], v[" #b0 ":" #b1 "], v96\n\t"
#define RDK(off) "ds_read_b128 v[84:87], v97 offset:" #off "\n\t"
#define RDV0(off) "ds_read_b128 v[88:91], v98 offset:" #off "\n\t"
#define RDV1(off) "ds_read_b128 v[92:95], v98 offset:" #off "\n\t"
#define PHASE_A(ko, vo0, vo1)   \
    EX(50) EX(51) EX(52) EX(53) WL2 QK(64, 79) RDK(ko) EX(54) EX(55) EX(56) EX(57) WL2 PV(88, 91, 48, 55) RDV0(vo0) \
    EX(58) EX(59) EX(60) EX(61) EX(62) EX(63) EX(64) EX(65) WL2 PV(92, 95, 56, 63) RDV1(vo1)
#define PHASE_B(ko, vo0, vo1)   \
    EX(66) EX(67) EX(68) EX(69) WL2 QK(48, 63) RDK(ko) EX(70) EX(71) EX(72) EX(73) WL2 PV(88, 91, 64, 71) RDV0(vo0) \
    EX(74) EX(75) EX(76) EX(77) EX(78) EX(79) EX(48) EX(49) WL2 PV(92, 95, 72, 79) RDV1(vo1)
DEV void attn_loop(f32x16& o, const f32x16& cin, const bf16x8& q, unsigned kaddr, unsigned vaddr, int niter, int barcnt) {
    int cnt = niter; int idx = 0xDDDD;
    asm volatile(
        "s_nop 1\n\t"
        RDK(0)
        "s_waitcnt lgkmcnt(0)\n\t"
        QK(48, 63)
        RDK(1024) RDV0(0) RDV1(528)
        "s_nop 8\n\t"
        EX(48) EX(49)
        "1:\n\t"
        PHASE_A(2048, 1056, 1584)
        PHASE_B(3072, 2112, 2640)
        PHASE_A(4096, 3168, 3696)
        PHASE_B(5120, 4224, 4752)
        "v_add_u32 v97, 0x1000, v97\n\t"
        "v_add_u32 v98, 0x1080, v98\n\t"
        "s_sub_u32 %[cnt], %[cnt], 1\n\t"
        "s_cmp_lg_u32 %[cnt], %[bar]\n\t"
        "s_cbranch_scc1 2f\n\t"
        "s_waitcnt vmcnt(0)\n\t"
        "s_barrier\n\t"
        "2:\n\t"
        "s_cmp_lg_u32 %[cnt], 0\n\t"
        "s_cbranch_scc1 1b\n\t"
        "s_waitcnt lgkmcnt(0)\n\t"
        "s_nop 15\n\t"
        : "+{v[16:31]}"(o), "+{v97}"(kaddr), "+{v98}"(vaddr), [cnt] "+s"(cnt)
        : "{v[32:47]}"(cin), "{v[80:83]}"(q), "{v96}"(idx), [bar] "s"(barcnt)
        : "v48", "v49", "v50", "v51", "v52", "v53", "v54", "v55", "v56", "v57", "v58", "v59", "v60", "v61", "v62", "v63",
          "v64", "v65", "v66", "v67", "v68", "v69", "v70", "v71", "v72", "v73", "v74", "v75", "v76", "v77", "v78", "v79",
          "v84", "v85", "v86", "v87", "v88", "v89", "v90", "v91", "v92", "v93", "v94", "v95", "scc", "memory");
}

DEV float sample_ref(LAS const char* kb, const bf16x8& q) {
    const f32x16 z = {};
    float m = max16(mfma(*(LAS const bf16x8*)kb, q, z), -3.0e38f);
    m = max16(mfma(*(LAS const bf16x8*)(kb + 1024), q, z), m);
    return halfmax(m) + MOFF;
}
struct Args {
    const uint16_t* x; const uint16_t* wq; const uint16_t* wk; const uint16_t* wv; const uint16_t* wo; const uint16_t* w1; const uint16_t* w2;
    char* ws; uint16_t* out;
};
DEV unsigned load_epoch(const char* ws) {
    unsigned epoch;
    asm volatile("s_load_dword %0, %1, 0x0\n\ts_waitcnt lgkmcnt(0)" : "=s"(epoch) : "s"(ws + WS_EPOCH) : "memory");
    return epoch;
}
DEV unsigned epoch_tag(unsigned epoch) { return epoch * 0x9E3779B1u + 0x7F4A7C15u; }
DEV void lds_add(LAS char* p) { __hip_atomic_fetch_add((LAS unsigned*)p, 1u, __ATOMIC_RELAXED, __HIP_MEMORY_SCOPE_WORKGROUP); }
DEV unsigned lds_add_rtn(LAS char* p) { return __hip_atomic_fetch_add((LAS unsigned*)p, 1u, __ATOMIC_RELAXED, __HIP_MEMORY_SCOPE_WORKGROUP); }
DEV unsigned lds_vol(LAS char* p) { return *(LAS volatile unsigned*)p; }
DEV void x_tile_dma(const uint16_t* x, int rb, int w, int l, unsigned sm0) {
#pragma unroll
    for (int i = 0; i < 4; ++i) {
        const int row = 4 * w + i;
        glds16s(x + (size_t)(rb * 64 + row) * DM, (unsigned)((l ^ (row & 15)) << 4), sm0 + L_X + row * 1024);
    }
}

DEV void g_role(const Args& A, LAS char* sm, unsigned sm0, int w, int l, int b, int rb, int j) {
    char* ws = A.ws;
    const int cu = rb * 8 + j, r32 = l & 31, h = l >> 5;
    x_tile_dma(A.x, rb, w, l, sm0);
    const int hcount = j < 3 ? 6 : 5, hstart = j < 3 ? 6 * j : 18 + 5 * (j - 3);
    const int gtype = w < 2 ? 0 : (w - 2 < hcount ? 1 : 2);
    const int krot = ROT ? 8 * (rb & 3) : 0;
    const uint16_t* wp = gtype == 0 ? A.wq + 64 * j + 32 * w : A.w1 + (hstart + (gtype == 1 ? w - 2 : 0)) * 32;
    const int ldw = gtype == 0 ? DM : FF;
    const unsigned wvo = (unsigned)((l >> 2) * ldw * 2 + (l & 3) * 16);
    u32x4 hpk[4];
    unsigned tag;
    if (gtype == 2) {
        tag = epoch_tag(load_epoch(ws));
        wait_vm<0>();
        barrier();
        if (l == 0) lds_add(sm + L_SYNC + 8);
    } else {
        const unsigned wst = sm0 + L_WR + w * (WRING * 1024);
        auto wdma = [&](int k) { glds16s(wp + (size_t)((k + krot) & 31) * 16 * ldw, wvo, wst + (k % WRING) * 1024); };
#pragma unroll
        for (int k = 0; k < WPRE; ++k) wdma(k);
        if (w == 0 && l == 0) *(LAS u32x4*)(sm + L_SYNC) = (u32x4){0u, 0u, 0u, 0u};
        tag = epoch_tag(load_epoch(ws));
        wait_vm<WPRE>();
        barrier();
        ST(1);
        f32x16 acc0 = {}, acc1 = {};
        f32x4 cs4 = {};
        LAS const char* xb = sm + L_X + r32 * 1024;
        const unsigned xo = (unsigned)((h ^ (r32 & 15)) << 4);
        LAS const char* wb = sm + L_WR + w * (WRING * 1024) + wfrag_lane_off(l);
        auto xoff = [&](int kk) -> unsigned { const unsigned kr = (unsigned)(kk + krot) & 31u; return (xo ^ ((kr & 7u) << 5)) + (kr >> 3) * 256u; };
        bf16x8 x0[2], x1[2], wf[2];
#pragma unroll
        for (int k = WPRE; k < WRING - 1; ++k) wdma(k);
        wait_vm<WRING - 2>();
        { const unsigned off = xoff(0); wf[0] = wfrag(wb); x0[0] = *(LAS const bf16x8*)(xb + off); x1[0] = *(LAS const bf16x8*)(xb + 32768 + off); }
        sfor<32>([&](auto K) {
            constexpr int k = K.v;
            if constexpr (k + WRING - 1 < 32) wdma(k + WRING - 1);
            if constexpr (k + 1 < 32) {
                constexpr int last = (k + WRING - 1 < 32) ? k + WRING - 1 : 31;
                wait_vm<last - (k + 1)>();
                constexpr int kk = k + 1;
                const unsigned off = xoff(kk);
                wf[kk & 1] = wfrag(wb + (kk % WRING) * 1024);
                x0[kk & 1] = *(LAS const bf16x8*)(xb + off); x1[kk & 1] = *(LAS const bf16x8*)(xb + 32768 + off);
            }
            SB();
            acc0 = mfma(wf[k & 1], x0[k & 1], acc0); acc1 = mfma(wf[k & 1], x1[k & 1], acc1);
            if (EXP_CS == 0) cs4 = lanesum8(__builtin_bit_cast(u32x4, wf[k & 1]), cs4);
            SB();
        });
        ST(2);
        if (l == 0) lds_add(sm + L_SYNC + 8);
        const float cs = xsum32(cs4[0]);
        LAS float* csb = (LAS float*)(sm + L_CS + w * 128);
        if (l < 32) csb[l] = cs;
        { unsigned sp = 0; while (lds_vol(sm + L_SYNC) < 4u || lds_vol(sm + L_SYNC + 4) < 2u) { __builtin_amdgcn_s_sleep(1); if (++sp > SPIN_CAP) break; } }
        wait_lgkm0();
        const f32x2 st0 = *(LAS const f32x2*)(sm + L_STATS + r32 * 8), st1 = *(LAS const f32x2*)(sm + L_STATS + (32 + r32) * 8);
        f32x4 csv[4];
#pragma unroll
        for (int i = 0; i < 4; ++i) csv[i] = *(LAS const f32x4*)(csb + 8 * i + 4 * h);
#pragma unroll
        for (int rt = 0; rt < 2; ++rt) {
            const f32x16& a = rt ? acc1 : acc0;
            const float mean = rt ? st1[0] : st0[0], rs = rt ? st1[1] : st0[1];
            float v[16];
#pragma unroll
            for (int i = 0; i < 16; ++i) v[i] = (a[i] - mean * csv[i >> 2][i & 3]) * rs;
            if (gtype == 0) {
                const float sc = 0.25f * 1.4426950408889634f;
#pragma unroll
                for (int p = 0; p < 2; ++p) {
                    u32x2 g0 = {pk2(v[8 * p] * sc, v[8 * p + 1] * sc), pk2(v[8 * p + 2] * sc, v[8 * p + 3] * sc)};
                    u32x2 g1 = {pk2(v[8 * p + 4] * sc, v[8 * p + 5] * sc), pk2(v[8 * p + 6] * sc, v[8 * p + 7] * sc)};
                    *(LAS u32x4*)(sm + L_Q + (rt * 4 + 2 * w + p) * 1024 + l * 16) = widen(g0, g1);
                }
            } else {
#pragma unroll
                for (int p = 0; p < 2; ++p) {
                    u32x2 g0 = {pk2(fmaxf(v[8 * p], 0.f), fmaxf(v[8 * p + 1], 0.f)), pk2(fmaxf(v[8 * p + 2], 0.f), fmaxf(v[8 * p + 3], 0.f))};
                    u32x2 g1 = {pk2(fmaxf(v[8 * p + 4], 0.f), fmaxf(v[8 * p + 5], 0.f)), pk2(fmaxf(v[8 * p + 6], 0.f), fmaxf(v[8 * p + 7], 0.f))};
                    hpk[rt * 2 + p] = widen(g0, g1);
                }
            }
        }
        ST(3);
    }
    barrier();
    ST(4);
    if (w == 0 && l < 12) *(LAS unsigned*)(sm + L_SYNC2 + 4 * l) = 0u;
    barrier();
    ST(5);
    {
        const char* src = ws + WS_KV; const unsigned lo = l * 16;
#pragma unroll
        for (int i = 0; i < 6; ++i) {
            glds16s_sc1(src + (16 + w + 8 * i) * 1024, lo, sm0 + (16 + w + 8 * i) * 1024);
            glds16s_sc1(src + (80 + w + 8 * i) * 1024, lo, sm0 + (80 + w + 8 * i) * 1024);
            if (GPACE && i < 5) __builtin_amdgcn_s_sleep(GPACE);
        }

        if (w < 2 && !EXP_NOGATHER) glds16s_sc1(src + (128 + w) * 1024, lo, sm0 + (128 + w) * 1024);
        if (gtype == 1 && !EXP_NOHST) {
            char* hb = ws + WS_H + ((size_t)(rb * 2) * HSTEPS + 2 * (hstart + w - 2)) * 1024 + l * 16;
#pragma unroll
            for (int rt = 0; rt < 2; ++rt)
#pragma unroll
                for (int p = 0; p < 2; ++p) st16_dev(hb + ((size_t)rt * HSTEPS + p) * 1024, hpk[rt * 2 + p]);
            wait_vm<4>();
        } else wait_vm<0>();
    }
    ST(7);
    barrier();
    ST(15);
    const int nt = w & 1, kq = w >> 1;
    const int k0 = kq < 2 ? 22 * kq : 44 + 21 * (kq - 2), nk = kq < 2 ? 22 : 21;
    const char* hp = ws + WS_H + ((size_t)(rb * 2) * HSTEPS + k0) * 1024;
    const uint16_t* w2p = A.w2 + (size_t)(k0 * 16) * DM + 64 * j + 32 * nt;
    wait_vm<0>();
    if (l == 0) { if (lds_add_rtn(sm + L_SYNC2 + 4) == 7u) st4_dev(ws + WS_FH + rb * 128 + j * 4, tag); }
    ST(14);
    if (w == 0) {
        for (unsigned sp = 0; sp < SPIN_CAP; ++sp) {
            const unsigned f = ld4_dev(ws + WS_FH + rb * 128 + (l & 7) * 4);
            if (__all(f == tag)) break;
            __builtin_amdgcn_s_sleep(HPOLL_SLEEP);
        }
        if (HACQ) { __builtin_amdgcn_fence(__ATOMIC_ACQUIRE, "agent"); wait_vm<0>(); }
        if (l == 0) *(LAS volatile unsigned*)(sm + L_SYNC2) = 1u;
    } else {
        unsigned sp = 0; while (lds_vol(sm + L_SYNC2) == 0u) { __builtin_amdgcn_s_sleep(8); if (++sp > SPIN_CAP) break; }
    }
    ST(13);
    f32x16 acc0 = {}, acc1 = {};
    {
        const int krot2 = ROT ? (XMAP ? (5 * (j & 3) + 3 * (rb & 7)) % 21 : (5 * (rb & 3) + 3 * j) % 21) : 0;
        const unsigned wvo2 = (unsigned)((l >> 2) * DM * 2 + (l & 3) * 16), lo = l * 16;
        LAS char* rsl = sm + L_RING2 + w * 3072;
        LAS const char* rb2 = rsl + wfrag_lane_off(l);
        u32x4 wr[FDW], xa[FDX], xb[FDX];
        auto kidx = [&](int k) { int kc = (k < 21 || k < nk) ? k : 20; kc += krot2; if (kc >= nk) kc -= nk; return kc; };
        auto issueW = [&](auto K) { constexpr int k = K.v; ld16s_async(wr[k % FDW], w2p + (size_t)kidx(k) * 16 * DM, wvo2); };
        auto issueX = [&](auto K) { constexpr int k = K.v; const int kc = kidx(k); if (HACQ) { ld16s_async(xa[k % FDX], hp + (size_t)kc * 1024, lo); ld16s_async(xb[k % FDX], hp + (size_t)(HSTEPS + kc) * 1024, lo); } else { ld16s_dev_async(xa[k % FDX], hp + (size_t)kc * 1024, lo); ld16s_dev_async(xb[k % FDX], hp + (size_t)(HSTEPS + kc) * 1024, lo); } };
        constexpr int DW = FDW - FDX;
        sfor<DW>([&](auto K) { issueW(K); });
        sfor<FDX>([&](auto K) { issueX(K); if constexpr (K.v + DW < 22) issueW(IC<K.v + DW>{}); });
        static_assert(DW >= 2, "W must run at least two steps ahead of X");
        auto younger = [](int k) constexpr { int n = (DW >= 3 && k + DW < 22) ? 1 : 0; for (int g = k + 1; g <= k + FDX - 1 && g < 22; ++g) n += 2 + ((g + DW < 22) ? 1 : 0); return n; };
        wait_vm<younger(0)>(); PIN(wr[0]); PIN(wr[1]);
        *(LAS u32x4*)(rsl + l * 16) = wr[0]; *(LAS u32x4*)(rsl + 1024 + l * 16) = wr[1];
        bf16x8 wf[2];
        wf[0] = wfrag(rb2);
        sfor<22>([&](auto K) {
            constexpr int k = K.v;
            if constexpr (k >= 1) wait_vm<younger(k)>();
            if constexpr (k + 2 < 22) { PIN(wr[(k + 2) % FDW]); *(LAS u32x4*)(rsl + ((k + 2) % 3) * 1024 + l * 16) = wr[(k + 2) % FDW]; }
            if constexpr (k + 1 < 22) wf[(k + 1) & 1] = wfrag(rb2 + ((k + 1) % 3) * 1024);
            PIN(xa[k % FDX]); PIN(xb[k % FDX]);
            SB();
            if (k < 21 || k < nk) { acc0 = mfma(wf[k & 1], __builtin_bit_cast(bf16x8, xa[k % FDX]), acc0); acc1 = mfma(wf[k & 1], __builtin_bit_cast(bf16x8, xb[k % FDX]), acc1); }
            SB();
            if constexpr (k + FDX < 22) issueX(IC<k + FDX>{});
            if constexpr (k + FDW < 22) issueW(IC<k + FDW>{});
            if constexpr (k == KSPLIT - 1) { ST(6); barrier(); ST(8); }
        });
    }
    const int fnt = w & 1, frt = (w >> 1) & 1, fp = (w >> 2) & 1;
    const size_t ro = (size_t)(rb * 64 + frt * 32 + r32) * DM + 64 * j + 32 * fnt + 16 * fp;
    const u32x2 x0 = *(const u32x2*)(A.x + ro + 4 * h), x1 = *(const u32x2*)(A.x + ro + 8 + 4 * h);
    if (KSPLIT > 22) { ST(6); barrier(); ST(8); }
    ST(7);
    {
        const uint16_t* wop = A.wo + (size_t)(kq * 128) * DM + 64 * j + 32 * nt;
        const unsigned wvo3 = (unsigned)((l >> 2) * DM * 2 + (l & 3) * 16);
#pragma unroll
        for (int s = 0; s < 8; ++s) glds16s(wop + (size_t)s * 16 * DM, wvo3, sm0 + L_WO + w * 8192 + s * 1024);
        if (w == 0) {
            for (unsigned sp = 0; sp < SPIN_CAP; ++sp) {
                const unsigned f = ld4_dev(ws + WS_FO + rb * 128 + (l & 7) * 4);
                if (__all(f == tag)) break;
                __builtin_amdgcn_s_sleep(OPOLL_SLEEP);
            }
        }
    }
    barrier();
    ST(9);
    {
        u32x4 oa[8], ob[8];
        const char* op = ws + WS_O + ((size_t)(rb * 2) * 32 + 8 * kq) * 1024;
#pragma unroll
        for (int s = 0; s < 8; ++s) { ld16s_dev_async(oa[s], op + s * 1024, l * 16); ld16s_dev_async(ob[s], op + (32 + s) * 1024, l * 16); }
        LAS const char* wb = sm + L_WO + w * 8192 + wfrag_lane_off(l);
        wait_vm<16>();
        bf16x8 wfo[2];
        wfo[0] = wfrag(wb);
        sfor<8>([&](auto S) {
            constexpr int s = S.v;
            if constexpr (s + 1 < 8) wfo[(s + 1) & 1] = wfrag(wb + (s + 1) * 1024);
            wait_vm<14 - 2 * s>(); PIN(oa[s]); PIN(ob[s]);
            SB();
            acc0 = mfma(wfo[s & 1], __builtin_bit_cast(bf16x8, oa[s]), acc0); acc1 = mfma(wfo[s & 1], __builtin_bit_cast(bf16x8, ob[s]), acc1);
            SB();
        });
        LAS f32x4* red = (LAS f32x4*)(sm + L_RED + w * 8192) + l;
#pragma unroll
        for (int i = 0; i < 4; ++i) {
            red[i * 64] = (f32x4){acc0[4 * i], acc0[4 * i + 1], acc0[4 * i + 2], acc0[4 * i + 3]};
            red[(4 + i) * 64] = (f32x4){acc1[4 * i], acc1[4 * i + 1], acc1[4 * i + 2], acc1[4 * i + 3]};
        }
    }
    ST(10);
    barrier();
    {
        f32x4 v0 = {}, v1 = {};
#pragma unroll
        for (int q4 = 0; q4 < 4; ++q4) {
            LAS const f32x4* red = (LAS const f32x4*)(sm + L_RED + (fnt + 2 * q4) * 8192) + l;
            v0 += red[(frt * 4 + 2 * fp) * 64]; v1 += red[(frt * 4 + 2 * fp + 1) * 64];
        }
        u32x2 g0 = {pk2(v0[0] + bflo(x0[0]), v0[1] + bfhi(x0[0])), pk2(v0[2] + bflo(x0[1]), v0[3] + bfhi(x0[1]))};
        u32x2 g1 = {pk2(v1[0] + bflo(x1[0]), v1[1] + bfhi(x1[0])), pk2(v1[2] + bflo(x1[1]), v1[3] + bfhi(x1[1]))};
        *(u32x4*)(A.out + ro + 8 * h) = widen(g0, g1);
    }
    ST(11);
}

DEV void a_role(const Args& A, LAS char* sm, unsigned sm0, int w, int l, int b, int rb, int j) {
    char* ws = A.ws;
    const int cu = rb * 8 + j, r32 = l & 31, h = l >> 5, a = w - 8, c16 = l & 15, g = l >> 4;
    x_tile_dma(A.x, rb, w, l, sm0);
    {
        const uint16_t* src = (a < 4 ? A.wk : A.wv) + (a & 3) * 2048;
#pragma unroll
        for (int i = 0; i < 4; ++i) glds16s(src + i * 512, l * 16, sm0 + (a < 4 ? L_WK : L_WV) + (a & 3) * 4096 + i * 1024);
    }
    u32x4 codepf = {};
    if (CODEPF && a == 3 && (b >> 3) < CODE_KB) {
        unsigned long long pc; asm volatile("s_getpc_b64 %0" : "=s"(pc));
        ld16s_async(codepf, (const char*)((pc - CODE_BACK) & ~255ull) + (b >> 3) * 1024, l * 16);
    }
    const unsigned epoch = load_epoch(ws), tag = epoch_tag(epoch);
    wait_vm<0>(); PIN(codepf);
    barrier();
    ST(1);
    __builtin_amdgcn_s_setprio(3);
    bf16x8 ones; for (int e = 0; e < 8; ++e) ones[e] = (__bf16)1.0f;
    if (a < 2) {
        const int g16 = j >> 1, xrow = 16 * g16 + c16;
        LAS const char* xb = sm + L_X + xrow * 1024;
        LAS const char* wb = sm + (a == 0 ? L_WK : L_WV) + (8 * g + (c16 >> 2)) * 32 + (l & 3) * 8;
        f32x4 d = {}, cs = {};
        auto chain = [&](auto AA) {
#pragma unroll
            for (int bt = 0; bt < 2; ++bt) {
                bf16x8 xf[8], wf[8];
#pragma unroll
                for (int s = 0; s < 8; ++s) {
                    const int ss = 8 * bt + s;
                    xf[s] = *(LAS const bf16x8*)(xb + (((4 * ss + g) ^ c16) << 4));
                    wf[s] = cat8(tr_read(wb + ss * 1024), tr_read(wb + ss * 1024 + 128));
                }
                SB();
#pragma unroll
                for (int s = 0; s < 8; ++s) {
                    if constexpr (AA.v == 0) { d = mfma16(wf[s], xf[s], d); cs = mfma16(wf[s], ones, cs); } else { d = mfma16(xf[s], wf[s], d); cs = mfma16(ones, wf[s], cs); }
                }
                SB();
            }
        };
        if (a == 0) chain(IC<0>{}); else chain(IC<1>{});
        PIN(d); PIN(cs);
        if (l == 0) lds_add(sm + L_SYNC + 12);
        { unsigned sp = 0; while (lds_vol(sm + L_SYNC) < 4u) { __builtin_amdgcn_s_sleep(1); if (++sp > SPIN_CAP) break; } }
        const int key = rb * 64 + 16 * g16;
        char* kv = ws + WS_KV;
        if (a == 0) {
            const f32x2 st = *(LAS const f32x2*)(sm + L_STATS + xrow * 8);
            const float mean = st[0], rs = st[1];
            u32x2 o = {pk2((d[0] - mean * cs[0]) * rs, (d[1] - mean * cs[1]) * rs), pk2((d[2] - mean * cs[2]) * rs, (d[3] - mean * cs[3]) * rs)};
            const int kk = key + c16;
            if ((c16 >> 3) == (j & 1)) st8_dev(kv + (kk >> 5) * 1024 + (g >> 1) * 512 + (kk & 31) * 16 + (g & 1) * 8, o);
        } else {
            float v[4];
#pragma unroll
            for (int r = 0; r < 4; ++r) { const f32x2 st = *(LAS const f32x2*)(sm + L_STATS + (16 * g16 + 4 * g + r) * 8); v[r] = (d[r] - st[0] * cs[r]) * st[1]; }
            u32x2 o = {pk2(v[0], v[1]), pk2(v[2], v[3])};
            char* vb = kv + KIMG + (key >> 4) * VSTEP;
            if ((g >> 1) == (j & 1)) st8_dev(vb + (j & 1) * 256 + c16 * 16 + (g & 1) * 8, o);
            if ((j & 1) == 0 && l < 2) st8_dev(vb + 512 + l * 8, (u32x2){0x3F803F80u, 0x3F803F80u});
        }
        wait_vm<0>();
        if (l == 0) { if (lds_add_rtn(sm + L_SYNC + 4) == 1u) st4_dev(ws + WS_FK + (KSPREAD ? rb * 128 + j * 4 : cu * 4), tag); }
        ST(2);
    } else if (a >= 4) {
        const int xrow = 16 * (a - 4) + c16;
        LAS const char* xb = sm + L_X + xrow * 1024;
        f32x4 m1 = {}, m2 = {};
        bf16x8 xf[16];
#pragma unroll
        for (int s = 0; s < 16; ++s) xf[s] = *(LAS const bf16x8*)(xb + (((4 * s + g) ^ c16) << 4));
        SB();
#pragma unroll
        for (int s = 0; s < 16; ++s) { m1 = mfma16(ones, xf[s], m1); m2 = mfma16(xf[s], xf[s], m2); }
        const float dg = (c16 & 2) ? ((c16 & 1) ? m2[3] : m2[2]) : ((c16 & 1) ? m2[1] : m2[0]);
        const float s2 = __shfl(dg, 16 * (c16 >> 2) + c16);
        const float mean = m1[0] * (1.f / DM), rs = __builtin_amdgcn_rsqf(fmaxf(s2 * (1.f / DM) - mean * mean, 0.f) + 1e-5f);
        if (l < 16) *(LAS f32x2*)(sm + L_STATS + xrow * 8) = (f32x2){mean, rs};
        wait_lgkm0();
        if (l == 0) lds_add(sm + L_SYNC);
        ST(2);
    } else if (a == 2) {
        { unsigned sp = 0; while (lds_vol(sm + L_SYNC + 8) < 8u) { __builtin_amdgcn_s_sleep(4); if (++sp > SPIN_CAP) break; } }
        const __amdgpu_buffer_rsrc_t rs = __builtin_amdgcn_make_buffer_rsrc((void*)(ws + WS_FK), 0, 4096, 0x00020000);
        for (unsigned sp = 0; sp < SPIN_CAP; ++sp) {
            asm volatile("" ::: "memory");
            const u32x4 fa = __builtin_amdgcn_raw_buffer_load_b128(rs, KSPREAD ? (l & 31) * 128 + (l >> 5) * 16 : l * 16, 0, 16);
            const bool ok = fa[0] == tag && fa[1] == tag && fa[2] == tag && fa[3] == tag;
            if (__all(ok)) break;
            __builtin_amdgcn_s_sleep(POLL_SLEEP);
        }
        if (b == 0 && l == 0) st4_dev(ws + WS_EPOCH, epoch + 1u);
        ST(2);
    }
    barrier();
    ST(4);
    bf16x8 q;
    {
        const char* src = ws + WS_KV; const unsigned lo = l * 16;
        glds16s_sc1(src + a * 1024, lo, sm0 + a * 1024);
        glds16s_sc1(src + (64 + a) * 1024, lo, sm0 + (64 + a) * 1024);
        glds16s_sc1(src + (8 + a) * 1024, lo, sm0 + (8 + a) * 1024);
        glds16s_sc1(src + (72 + a) * 1024, lo, sm0 + (72 + a) * 1024);
        q = *(LAS const bf16x8*)(sm + L_Q + a * 1024 + l * 16);
        wait_vm<0>();
    }
    barrier();
    ST(5);
    const int head = 4 * j + (a & 3), qh = a >> 2;
    __builtin_amdgcn_s_setprio(APRIO);
    LAS const char* kb = sm + l * 16;
    LAS const char* vb = sm + KIMG + (r32 < 16 ? h * 256 + r32 * 16 : 512);
    f32x16 o;
    {
        float mref = sample_ref(kb, q);
        for (int attempt = 0;; ++attempt) {
            f32x16 cin;
#pragma unroll
            for (int i = 0; i < 16; ++i) { cin[i] = -mref; o[i] = 0.f; }
            attn_loop(o, cin, q, (unsigned)(uintptr_t)kb, (unsigned)(uintptr_t)vb, 16, attempt == 0 ? 14 : -1);
            bool bad = !(o[8] < 1.0e30f) || !(o[8] > 0.f);
#pragma unroll
            for (int i = 0; i < 8; ++i) bad = bad || !(fabsf(o[i]) < 3.0e38f);
            if (attempt > 0 || !__any(bad)) break;
            float m = -3.0e38f; const f32x16 z = {};
            for (int t = 0; t < 64; ++t) m = max16(mfma(*(LAS const bf16x8*)(kb + t * 1024), q, z), m);
            mref = halfmax(m);
        }
    }
    ST(6);
    const float r = 1.0f / o[8];
    u32x2 g0 = {pk2(o[0] * r, o[1] * r), pk2(o[2] * r, o[3] * r)}, g1 = {pk2(o[4] * r, o[5] * r), pk2(o[6] * r, o[7] * r)};
    st16_dev(ws + WS_O + ((size_t)(rb * 2 + qh) * 32 + head) * 1024 + l * 16, widen(g0, g1));
    barrier();
    ST(8);
    wait_vm<0>();
    if (l == 0) { if (lds_add_rtn(sm + L_SYNC2 + 8) == 7u) st4_dev(ws + WS_FO + rb * 128 + j * 4, tag); }
    ST(7);
    barrier();
    barrier();
    ST(11);
}

__global__ __launch_bounds__(1024, 4) void fused_kernel(const uint16_t* __restrict__ x, const uint16_t* __restrict__ wq, const uint16_t* __restrict__ wk,
                                                        const uint16_t* __restrict__ wv, const uint16_t* __restrict__ wo, const uint16_t* __restrict__ w1,
                                                        const uint16_t* __restrict__ w2, char* __restrict__ ws, uint16_t* __restrict__ out) {
    __shared__ __attribute__((aligned(16))) char smem[LDS_BYTES];
    const int tid = threadIdx.x, l = tid & 63, w = __builtin_amdgcn_readfirstlane(tid >> 6);
    const int b = blockIdx.x, xcd = b & 7, idx = b >> 3;
    const int rb = XMAP ? (xcd >> 1) * 8 + (idx >> 2) : xcd * 4 + (idx >> 3), j = XMAP ? (xcd & 1) * 4 + (idx & 3) : idx & 7;
    const unsigned sm0 = (unsigned)(uintptr_t)smem;
    LAS char* sm = (LAS char*)smem;
    ST(0);
    const Args A = {x, wq, wk, wv, wo, w1, w2, ws, out};
    if (w < 8) g_role(A, sm, sm0, w, l, b, rb, j); else a_role(A, sm, sm0, w, l, b, rb, j);
}

extern "C" void kernel_launch(void* const* d_in, const int* in_sizes, int n_in, void* d_out, int out_size, void* d_ws, size_t ws_size,
                              hipStream_t stream) {
    fused_kernel<<<256, 1024, 0, stream>>>((const uint16_t*)d_in[0], (const uint16_t*)d_in[1], (const uint16_t*)d_in[2], (const uint16_t*)d_in[3],
                                           (const uint16_t*)d_in[4], (const uint16_t*)d_in[6], (const uint16_t*)d_in[7], (char*)d_ws, (uint16_t*)d_out);
}
```
